```python
import torch

def _softmax(x, axis=-1):
    return torch.softmax(x, dim=axis)
def _gelu(x):
    return torch.nn.functional.gelu(x, approximate="tanh")
def _silu(x):
    return torch.nn.functional.silu(x)
def _sigmoid(x):
    return torch.sigmoid(x)
def _layernorm(x, axis=-1, eps=1e-5):
    m = x.mean(dim=axis, keepdim=True)
    v = x.var(dim=axis, keepdim=True, unbiased=False)
    return (x - m) / torch.sqrt(v + eps)
def _scatter_add(tgt, idx, src, axis=0):
    return tgt.clone().index_add_(axis, idx, src)
def _segment_sum(src, seg, n):
    out = torch.zeros((n,) + src.shape[1:], dtype=src.dtype)
    return out.index_add_(0, seg, src)
def _pad(x, pw):
    flat = tuple(v for pair in reversed(pw) for v in pair)
    return torch.nn.functional.pad(x, flat)

kernel_name = 'gated_mix__spectral_fnet__mha_block__prompt'

def setup_inputs():

    _w = (lambda a: (lambda t: t.to(torch.bfloat16) if t.is_floating_point() else t)(torch.as_tensor(a)))
    def _A_setup():
        B, S, D = 1, 2048, 512
        ns = torch.arange(S)[:, None] * torch.arange(S)[None, :]
        nd = torch.arange(D)[:, None] * torch.arange(D)[None, :]
        Ws_re = torch.cos(-2.0 * torch.pi * ns / S).to(float)
        Ws_im = torch.sin(-2.0 * torch.pi * ns / S).to(float)
        Wd_re = torch.cos(-2.0 * torch.pi * nd / D).to(float)
        Wd_im = torch.sin(-2.0 * torch.pi * nd / D).to(float)
        return {
            "x": _w((lambda *s: torch.randn(*s, dtype=torch.bfloat16) * 0.05)(B, S, D)),
            "dft_seq_re": _w(Ws_re),
            "dft_seq_im": _w(Ws_im),
            "dft_hid_re": _w(Wd_re),
            "dft_hid_im": _w(Wd_im),
        }
    _A_in = _A_setup()

    def _B_setup():
        B, S, D, H = 1, 2048, 512, 32
        return {
            "x": _w((lambda *s: torch.randn(*s, dtype=torch.bfloat16) * 0.05)(B, S, D)),
            "wqkv": _w((lambda *s: torch.randn(*s, dtype=torch.bfloat16) * 1.6)(D, 3 * D)),
            "wo": _w((lambda *s: torch.randn(*s, dtype=torch.bfloat16) * 1.6)(D, D)),
        }
    _B_in = _B_setup()

    return {
        "A_x": _A_in["x"],
        "A_dft_seq_re": _A_in["dft_seq_re"],
        "A_dft_seq_im": _A_in["dft_seq_im"],
        "A_dft_hid_re": _A_in["dft_hid_re"],
        "A_dft_hid_im": _A_in["dft_hid_im"],
        "B_x": _B_in["x"],
        "B_wqkv": _B_in["wqkv"],
        "B_wo": _B_in["wo"],
        "gate_w": _w((lambda *s: torch.randn(*s, dtype=torch.bfloat16) * 0.05)(512) * 16.0),
    }

def reference(A_x, A_dft_seq_re, A_dft_seq_im, A_dft_hid_re, A_dft_hid_im, B_x, B_wqkv, B_wo, gate_w):
    def _A_ref(x, dft_seq_re, dft_seq_im, dft_hid_re, dft_hid_im):
        a = torch.einsum("bsd,ed->bse", x, dft_hid_re)
        b = torch.einsum("bsd,ed->bse", x, dft_hid_im)
        y = torch.einsum("ts,bse->bte", dft_seq_re, a) - torch.einsum("ts,bse->bte", dft_seq_im, b)
        return y

    def _B_ref(x, wqkv, wo):
        B, S, D = x.shape
        H = 32
        Dh = D // H
        qkv = (lambda x, *d: x.permute(*d))((x @ wqkv).reshape(B, S, 3, H, Dh), 2, 0, 3, 1, 4)
        q, k, v = qkv[0], qkv[1], qkv[2]
        scores = torch.einsum("bhqd,bhkd->bhqk", q, k) * (Dh ** -0.5)
        p = _softmax(scores, axis=-1)
        o = torch.einsum("bhqk,bhkd->bhqd", p, v)
        return (lambda x, *d: x.permute(*d))(o, 0, 2, 1, 3).reshape(B, S, D) @ wo

    x = A_x
    g = _sigmoid((x * gate_w).sum(-1, keepdims=True))
    a = _A_ref(x, A_dft_seq_re, A_dft_seq_im, A_dft_hid_re, A_dft_hid_im)
    b = _B_ref(x, B_wqkv, B_wo)
    return g * a + (1 - g) * b
```

```cpp
#include <hip/hip_runtime.h>
#include <hip/hip_bf16.h>
#include <stdint.h>

typedef __bf16 bf16x2 __attribute__((ext_vector_type(2)));
typedef __bf16 bf16x8 __attribute__((ext_vector_type(8)));
typedef __bf16 bf16x16 __attribute__((ext_vector_type(16)));
typedef short s16x4 __attribute__((ext_vector_type(4)));
typedef short s16x8 __attribute__((ext_vector_type(8)));
typedef float f32x2 __attribute__((ext_vector_type(2)));
typedef float f32x4 __attribute__((ext_vector_type(4)));
typedef float f32x8 __attribute__((ext_vector_type(8)));
typedef float f32x16 __attribute__((ext_vector_type(16)));
typedef unsigned u32x4 __attribute__((ext_vector_type(4)));
typedef unsigned short u16;
#define DI __device__ __forceinline__
#define LDSP __attribute__((address_space(3)))

constexpr int NP = 4;
constexpr size_t WS_Q = 0, WS_KV = 2u << 20, WS_AB = 6u << 20, WS_WO = 10u << 20, WS_O = 11u << 20, WS_G = 13u << 20, WS_Y = 14u << 20;
constexpr float QSCALE = 0.25f * 1.4426950408889634f;

DI unsigned pk2(float lo, float hi) { f32x2 v = {lo, hi}; return __builtin_bit_cast(unsigned, __builtin_convertvector(v, bf16x2)); }
DI float bf2f(unsigned short b) { return __builtin_bit_cast(float, (unsigned)b << 16); }
DI float rbf(float x) { return bf2f((u16)(pk2(x, 0.f) & 0xffffu)); }
DI f32x16 mfma(bf16x8 a, bf16x8 b, f32x16 c) { return __builtin_amdgcn_mfma_f32_32x32x16_bf16(a, b, c, 0, 0, 0); }
DI void dma16(unsigned voff, const void* sbase, unsigned ldsaddr) {
  unsigned keep;
  asm volatile("s_mov_b32 %0, m0\n\ts_mov_b32 m0, %3\n\ts_nop 0\n\tglobal_load_lds_dwordx4 %1, %2\n\ts_mov_b32 m0, %0"
               : "=&s"(keep) : "v"(voff), "s"(sbase), "s"(ldsaddr) : "memory");
}
template <int N> DI void wait_bar() { asm volatile("s_waitcnt vmcnt(%0) lgkmcnt(0)\n\ts_barrier" ::"i"(N) : "memory"); }
DI bf16x8 lds_b128(unsigned addr) { return *(const LDSP bf16x8*)(uintptr_t)addr; }
DI bf16x8 lds_tr2(unsigned addr) {
  s16x4 lo = __builtin_amdgcn_ds_read_tr16_b64_v4i16((LDSP s16x4*)(uintptr_t)addr);
  s16x4 hi = __builtin_amdgcn_ds_read_tr16_b64_v4i16((LDSP s16x4*)(uintptr_t)(addr + 256));
  s16x8 v = __builtin_shufflevector(lo, hi, 0, 1, 2, 3, 4, 5, 6, 7);
  return __builtin_bit_cast(bf16x8, v);
}
DI u32x4 pack8(const f32x16& a, int o, float s) {
  u32x4 r = {pk2(a[o] * s, a[o + 1] * s), pk2(a[o + 2] * s, a[o + 3] * s), pk2(a[o + 4] * s, a[o + 5] * s), pk2(a[o + 6] * s, a[o + 7] * s)};
  return r;
}
DI u32x4 swap_halves(u32x4 v) {
  auto x = __builtin_amdgcn_permlane32_swap(v[0], v[2], false, false);
  auto y = __builtin_amdgcn_permlane32_swap(v[1], v[3], false, false);
  u32x4 r = {x[0], y[0], x[1], y[1]};
  return r;
}

constexpr unsigned K1_STAGE = 36864, K1_W = 16384, K1_D = 28672;
template <int NDMA, class F, class C> DI void k1_loop(F issue, C compute) {
  issue(0); issue(1); issue(2); issue(3);
  wait_bar<3 * NDMA>();           compute(0);
  wait_bar<2 * NDMA>(); issue(4); compute(1);
  wait_bar<2 * NDMA>(); issue(5); compute(2);
  wait_bar<2 * NDMA>(); issue(6); compute(3);
  wait_bar<2 * NDMA>(); issue(7); compute(4);
  wait_bar<2 * NDMA>();           compute(5);
  wait_bar<NDMA>();               compute(6);
  wait_bar<0>();                  compute(7);
}
__global__ __launch_bounds__(512, 2) void k1_proj(const char* x, const char* wqkv, const char* dre, const char* dim, const u16* wo,
                                                  const char* gate_w, char* ws) {
  extern __shared__ __attribute__((aligned(16))) char lds[];
  const int lane = threadIdx.x & 63, w = __builtin_amdgcn_readfirstlane(threadIdx.x >> 6), r = lane & 31, h = lane >> 5;
  const int b = blockIdx.x, xcd = b & 7, i = b >> 3, mt = 8 * (xcd & 1) + (i & 7), nt = 4 * (xcd >> 1) + (i >> 3);
  const unsigned l0 = (unsigned)(uintptr_t)lds;
  const u32x4 gx = *(const u32x4*)(x + (size_t)(8 * b + w) * 1024 + lane * 16);
  const u32x4 gw = *(const u32x4*)(gate_w + lane * 16);
  unsigned wv[8];
  if (w < 2) {
    const int f = 2 * b + w;
    const u16* p = wo + (size_t)((f & 31) * 16 + 4 * h) * 512 + (f >> 5) * 32 + r;
#pragma unroll
    for (int j = 0; j < 8; ++j) wv[j] = p[(8 * (j >> 2) + (j & 3)) * 512];
  }
  const char* xb = x + (size_t)mt * 128 * 1024;
  const char* db = (nt < 8 ? dre : dim) + (size_t)(nt & 7) * 64 * 1024;
  const char* wb = wqkv + nt * 64;
  const int rr = lane >> 3, pc = lane & 7;
  const unsigned vx0 = (16 * w + rr) * 1024 + ((pc ^ ((rr >> 1) & 7)) * 16);
  const unsigned vx1 = (16 * w + 8 + rr) * 1024 + ((pc ^ (((rr >> 1) + 4) & 7)) * 16);
  const unsigned vd = (8 * w + rr) * 1024 + ((pc ^ (((rr >> 1) + 4 * w) & 7)) * 16);
  const int rot = (mt + 2 * nt) & 7;
  const int tw = w & 3, tt = 4 * mt + tw;
  unsigned ax[4];
#pragma unroll
  for (int kk = 0; kk < 4; ++kk) ax[kk] = l0 + (32 * tw + r) * 128 + (((2 * kk + h) ^ ((r >> 1) & 7)) * 16);
  if (w < 4) {
    const unsigned vw = (16 * w + (lane >> 2)) * 3072 + (lane & 3) * 16;
    auto issue = [&](int s) {
      const int kc = (s + rot) & 7; const unsigned l = l0 + (s & 3) * K1_STAGE;
      dma16(vx0, xb + kc * 128, l + w * 2048); dma16(vx1, xb + kc * 128, l + w * 2048 + 1024);
      dma16(vw, wb + kc * 64 * 3072, l + K1_W + w * 1024);
      dma16(vd, db + kc * 128, l + K1_D + w * 1024);
    };
    f32x16 aq = {}, ak = {}, av = {};
    const unsigned aw = l0 + K1_W + (8 * h + ((lane & 15) >> 2)) * 64 + ((lane >> 4) & 1) * 32 + (lane & 3) * 8;
    auto compute = [&](int s) {
      const unsigned so = (s & 3) * K1_STAGE;
#pragma unroll
      for (int kk = 0; kk < 4; ++kk) {
        const bf16x8 xf = lds_b128(ax[kk] + so);
        aq = mfma(lds_tr2(aw + so + kk * 1024), xf, aq);
        ak = mfma(lds_tr2(aw + so + kk * 1024 + 4096), xf, ak);
        av = mfma(xf, lds_tr2(aw + so + kk * 1024 + 8192), av);
      }
    };
    k1_loop<4>(issue, compute);
    const int pr = (r & 19) | ((r & 4) << 1) | ((r & 8) >> 1);
#pragma unroll
    for (int hh = 0; hh < 2; ++hh) {
      const size_t ht = (size_t)((2 * nt + hh) * 64 + tt);
      *(u32x4*)(ws + WS_Q + ht * 1024 + lane * 16) = pack8(aq, 8 * hh, QSCALE);
      *(u32x4*)(ws + WS_KV + ht * 2048 + (h * 32 + pr) * 16) = pack8(ak, 8 * hh, 1.f);
    }
    char* vp = ws + WS_KV + (size_t)((2 * nt + (r >> 4)) * 64 + tt) * 2048 + 1024 + h * 256 + (r & 15) * 16;
    *(u32x4*)(vp) = pack8(av, 0, 1.f);
    *(u32x4*)(vp + 512) = pack8(av, 8, 1.f);
  } else {
    const int q0 = 4 + 2 * tw;
    const unsigned vw0 = (16 * (q0 & 3) + (lane >> 2)) * 3072 + (q0 >> 2) * 1024 + (lane & 3) * 16;
    auto issue = [&](int s) {
      const int kc = (s + rot) & 7; const unsigned l = l0 + (s & 3) * K1_STAGE;
      dma16(vx0, xb + kc * 128, l + w * 2048); dma16(vx1, xb + kc * 128, l + w * 2048 + 1024);
      dma16(vw0, wb + kc * 64 * 3072, l + K1_W + q0 * 1024); dma16(vw0, wb + kc * 64 * 3072 + 16 * 3072, l + K1_W + q0 * 1024 + 1024);
      dma16(vd, db + kc * 128, l + K1_D + w * 1024);
    };
    f32x16 a0 = {}, a1 = {};
    unsigned ad[4];
#pragma unroll
    for (int kk = 0; kk < 4; ++kk) ad[kk] = l0 + K1_D + r * 128 + (((2 * kk + h) ^ ((r >> 1) & 7)) * 16);
    auto compute = [&](int s) {
      const unsigned so = (s & 3) * K1_STAGE;
#pragma unroll
      for (int kk = 0; kk < 4; ++kk) {
        const bf16x8 xf = lds_b128(ax[kk] + so);
        a0 = mfma(xf, lds_b128(ad[kk] + so), a0);
        a1 = mfma(xf, lds_b128(ad[kk] + so + 4096), a1);
      }
    };
    k1_loop<5>(issue, compute);
    char* ab = ws + WS_AB + ((size_t)((nt >> 3) * 16 + 2 * (nt & 7)) * 128 + 2 * tt) * 1024 + lane * 16;
    *(u32x4*)(ab) = swap_halves(pack8(a0, 0, 1.f));
    *(u32x4*)(ab + 1024) = swap_halves(pack8(a0, 8, 1.f));
    *(u32x4*)(ab + 131072) = swap_halves(pack8(a1, 0, 1.f));
    *(u32x4*)(ab + 131072 + 1024) = swap_halves(pack8(a1, 8, 1.f));
  }
  float z = 0.f;
#pragma unroll
  for (int j = 0; j < 4; ++j) {
    z += rbf(bf2f((u16)(gx[j] & 0xffff)) * bf2f((u16)(gw[j] & 0xffff)));
    z += rbf(bf2f((u16)(gx[j] >> 16)) * bf2f((u16)(gw[j] >> 16)));
  }
#pragma unroll
  for (int o = 32; o; o >>= 1) z += __shfl_xor(z, o);
  if (lane == 0) ((float*)(ws + WS_G))[8 * b + w] = rbf(1.f / (1.f + __expf(-rbf(z))));
  if (w < 2) {
    u32x4 v = {wv[0] | (wv[1] << 16), wv[2] | (wv[3] << 16), wv[4] | (wv[5] << 16), wv[6] | (wv[7] << 16)};
    *(u32x4*)(ws + WS_WO + (size_t)(2 * b + w) * 1024 + lane * 16) = v;
  }
}

DI f32x16 smf(bf16x8 a, const f32x16& p, int o, f32x16 c) {
  f32x8 t = o ? __builtin_shufflevector(p, p, 8, 9, 10, 11, 12, 13, 14, 15) : __builtin_shufflevector(p, p, 0, 1, 2, 3, 4, 5, 6, 7);
  return __builtin_amdgcn_smfmac_f32_32x32x32_bf16(a, __builtin_bit_cast(bf16x16, t), c, 0xdddd, 0, 0);
}
DI float rowmax16(const f32x16& s) {
  float m = s[0];
#pragma unroll
  for (int j = 1; j < 16; ++j) m = fmaxf(m, s[j]);
  float m2 = m;
  asm volatile("s_nop 1\n\tv_permlane32_swap_b32 %0, %1" : "+v"(m), "+v"(m2));
  return fmaxf(m, m2);
}
DI f32x16 splat16(float v) { f32x16 r; for (int j = 0; j < 16; ++j) r[j] = v; return r; }
DI f32x16 attn_true_max(const char* kv, bf16x8 q, int lane) {
  const int r = lane & 31, h = lane >> 5;
  const s16x8 one = {0x3f80, 0x3f80, 0x3f80, 0x3f80, 0x3f80, 0x3f80, 0x3f80, 0x3f80};
  float m = -INFINITY;
  for (int t = 0; t < 64; ++t) m = fmaxf(m, rowmax16(mfma(*(const bf16x8*)(kv + t * 2048 + lane * 16), q, f32x16{})));
  const f32x16 nm = splat16(-m);
  f32x16 o = {};
  for (int t = 0; t < 64; ++t) {
    f32x16 s = mfma(*(const bf16x8*)(kv + t * 2048 + lane * 16), q, nm);
    const char* vp = kv + t * 2048 + 1024 + h * 256 + (r & 15) * 16;
    bf16x8 v0 = *(const bf16x8*)vp, v1 = *(const bf16x8*)(vp + 512);
    if (r >= 16) { v0 = __builtin_bit_cast(bf16x8, one); v1 = v0; }
#pragma unroll
    for (int j = 0; j < 16; ++j) s[j] = __builtin_amdgcn_exp2f(s[j]);
    o = smf(v0, s, 0, o); o = smf(v1, s, 1, o);
  }
  return o;
}
__global__ __launch_bounds__(512, 2) void k2_attn(char* ws) {
  extern __shared__ __attribute__((aligned(16))) char lds[];
  const int lane = threadIdx.x & 63, w = __builtin_amdgcn_readfirstlane(threadIdx.x >> 6), r = lane & 31, h = lane >> 5;
  const int b = blockIdx.x, xcd = b & 7, i = b >> 3, head = xcd * 4 + (i >> 3), qb = i & 7, qs = w & 3, kh = w >> 2;
  const unsigned l0 = (unsigned)(uintptr_t)lds;
  const char* kv = ws + WS_KV + (size_t)head * 131072;
  const int qt = qb * 8 + qs * 2;
  const char* qp = ws + WS_Q + (size_t)(head * 64 + qt) * 1024 + lane * 16;
  const bf16x8 q0 = *(const bf16x8*)qp, q1 = *(const bf16x8*)(qp + 1024);
  const bf16x8 k0 = *(const bf16x8*)(kv + lane * 16);
#pragma unroll
  for (int j = 0; j < 16; ++j) dma16(lane * 16, kv + (j * 8 + w) * 1024, l0 + (j * 8 + w) * 1024);
  const f32x16 nm0 = splat16(-rowmax16(mfma(k0, q0, f32x16{}))), nm1 = splat16(-rowmax16(mfma(k0, q1, f32x16{})));
  wait_bar<0>();
  f32x16 o0 = {}, o1 = {};
  const s16x8 one = {0x3f80, 0x3f80, 0x3f80, 0x3f80, 0x3f80, 0x3f80, 0x3f80, 0x3f80};
  const unsigned ka = l0 + kh * 65536 + lane * 16, va = l0 + kh * 65536 + 1024 + h * 256 + (r & 15) * 16;
#pragma unroll 2
  for (int t = 0; t < 32; ++t) {
    const bf16x8 kf = lds_b128(ka + t * 2048);
    bf16x8 v0 = lds_b128(va + t * 2048), v1 = lds_b128(va + t * 2048 + 512);
    if (r >= 16) { v0 = __builtin_bit_cast(bf16x8, one); v1 = v0; }
    f32x16 s0 = mfma(kf, q0, nm0), s1 = mfma(kf, q1, nm1);
#pragma unroll
    for (int j = 0; j < 16; ++j) s0[j] = __builtin_amdgcn_exp2f(s0[j]);
    o0 = smf(v0, s0, 0, o0); o0 = smf(v1, s0, 1, o0);
#pragma unroll
    for (int j = 0; j < 16; ++j) s1[j] = __builtin_amdgcn_exp2f(s1[j]);
    o1 = smf(v0, s1, 0, o1); o1 = smf(v1, s1, 1, o1);
  }
  __syncthreads();
  float* ex = (float*)lds + w * 9 * 64 + lane;
#pragma unroll
  for (int j = 0; j < 9; ++j) ex[j * 64] = kh ? o0[j] : o1[j];
  __syncthreads();
  const float* ey = (const float*)lds + (w ^ 4) * 9 * 64 + lane;
  f32x16 o = kh ? o1 : o0;
  bool ok = true;
#pragma unroll
  for (int j = 0; j < 9; ++j) { o[j] += ey[j * 64]; ok = ok && (__builtin_fabsf(o[j]) < 3.0e38f); }
  ok = ok && o[8] > 0.25f;
  if (!__all(ok)) o = attn_true_max(kv, kh ? q1 : q0, lane);
  *(u32x4*)(ws + WS_O + (size_t)((qt + kh) * 32 + head) * 1024 + lane * 16) = pack8(o, 0, 1.f / o[8]);
}

__global__ __launch_bounds__(512, 2) void kg3(const char* cre, const char* cim, char* ws) {
  const int lane = threadIdx.x & 63, w = __builtin_amdgcn_readfirstlane(threadIdx.x >> 6), r = lane & 31, h = lane >> 5;
  const int b = blockIdx.x, xcd = b & 7, i = b >> 3;
  const int np = xcd >> 1, p = np >> 1, k2 = np & 1, tb = (xcd & 1) * 8 + (i >> 2), eb = i & 3;
  const int et = eb * 4 + (w & 1) * 2, tt = tb * 4 + (w >> 1);
  const char* cp = (p ? cim : cre) + (size_t)(tt * 32 + r) * 4096 + k2 * 2048 + h * 16;
  const char* ap = ws + WS_AB + ((size_t)(p * 16 + et) * 128 + k2 * 64) * 1024 + lane * 16;
  f32x16 a0 = {}, a1 = {};
#pragma unroll 4
  for (int ks = 0; ks < 64; ++ks) {
    const bf16x8 cf = *(const bf16x8*)(cp + ks * 32);
    a0 = mfma(*(const bf16x8*)(ap + ks * 1024), cf, a0);
    a1 = mfma(*(const bf16x8*)(ap + 131072 + ks * 1024), cf, a1);
  }
  char* yp = ws + WS_Y + ((size_t)(np * 16 + et) * 64 + tt) * 2048 + lane * 16;
  *(u32x4*)(yp) = pack8(a0, 0, 1.f); *(u32x4*)(yp + 1024) = pack8(a0, 8, 1.f);
  *(u32x4*)(yp + 131072) = pack8(a1, 0, 1.f); *(u32x4*)(yp + 131072 + 1024) = pack8(a1, 8, 1.f);
}

__global__ __launch_bounds__(512, 2) void k3_out(const char* ws, char* out) {
  extern __shared__ __attribute__((aligned(16))) char lds[];
  const int lane = threadIdx.x & 63, w = __builtin_amdgcn_readfirstlane(threadIdx.x >> 6), r = lane & 31, h = lane >> 5;
  const int b = blockIdx.x, xcd = b & 7, i = b >> 3, tb = xcd * 4 + (i >> 3), eb = i & 7;
  const int ei = w & 1, ti = (w >> 1) & 1, kh = w >> 2, et = 2 * eb + ei, tt = 2 * tb + ti;
  const unsigned l0 = (unsigned)(uintptr_t)lds;
  const char* src = (w < 4 ? ws + WS_WO + (size_t)(2 * eb + ((w >> 1) & 1)) * 32768 : ws + WS_O + (size_t)(2 * tb + ((w >> 1) & 1)) * 32768) + (w & 1) * 16384;
#pragma unroll
  for (int c = 0; c < 4; ++c)
#pragma unroll
    for (int j = 0; j < 4; ++j) dma16(lane * 16, src + (c * 4 + j) * 1024, l0 + c * 32768 + w * 4096 + j * 1024);
  u32x4 y[NP];
  float g;
  {
    const char* yp = ws + WS_Y + ((size_t)et * 64 + tt) * 2048 + kh * 1024;
    const char* gp = ws + WS_G + tt * 128;
#pragma unroll
    for (int j = 0; j < NP; ++j) asm volatile("global_load_dwordx4 %0, %1, %2" : "=v"(y[j]) : "v"(lane * 16), "s"(yp + (size_t)j * 2097152) : "memory");
    asm volatile("global_load_dword %0, %1, %2" : "=v"(g) : "v"(r * 4), "s"(gp) : "memory");
  }
  f32x16 acc = {};
  const unsigned aa = l0 + (ei * 2 + kh) * 4096 + lane * 16, ba = l0 + 16384 + (ti * 2 + kh) * 4096 + lane * 16;
  auto compute = [&](int c) {
#pragma unroll
    for (int j = 0; j < 4; ++j) acc = mfma(lds_b128(aa + c * 32768 + j * 1024), lds_b128(ba + c * 32768 + j * 1024), acc);
  };
  wait_bar<12 + NP + 1>(); compute(0);
  wait_bar<8 + NP + 1>(); compute(1);
  wait_bar<4 + NP + 1>(); compute(2);
  wait_bar<NP + 1>(); compute(3);
  f32x4* ex = (f32x4*)(lds + 131072 + w * 2048) + lane;
  { f32x4 u = kh ? f32x4{acc[0], acc[1], acc[2], acc[3]} : f32x4{acc[8], acc[9], acc[10], acc[11]};
    f32x4 v = kh ? f32x4{acc[4], acc[5], acc[6], acc[7]} : f32x4{acc[12], acc[13], acc[14], acc[15]};
    ex[0] = u; ex[64] = v; }
  asm volatile("s_waitcnt vmcnt(0) lgkmcnt(0)\n\ts_barrier" : "+v"(y[0]), "+v"(y[1]), "+v"(y[NP - 2]), "+v"(y[NP - 1]), "+v"(g)::"memory");
  const f32x4* ey = (const f32x4*)(lds + 131072 + (w ^ 4) * 2048) + lane;
  const f32x4 u = ey[0], v = ey[64];
  const float g1 = rbf(1.f - g);
  float res[8];
#pragma unroll
  for (int j = 0; j < 8; ++j) {
    const float pr = (kh ? acc[8 + j] : acc[j]) + (j < 4 ? u[j & 3] : v[j & 3]);
    float yy = 0.f;
#pragma unroll
    for (int n = 0; n < NP; ++n) {
      const float t = bf2f((u16)((j & 1) ? (y[n][j >> 1] >> 16) : (y[n][j >> 1] & 0xffff)));
      yy = (n < NP / 2) ? yy + t : yy - t;
    }
    res[j] = g * yy + g1 * pr;
  }
  u32x4 pk = {pk2(res[0], res[1]), pk2(res[2], res[3]), pk2(res[4], res[5]), pk2(res[6], res[7])};
  *(u32x4*)(out + (size_t)(tt * 32 + r) * 1024 + (et * 32 + kh * 16 + h * 8) * 2) = swap_halves(pk);
}

extern "C" void kernel_launch(void* const* d_in, const int* in_sizes, int n_in, void* d_out, int out_size, void* d_ws, size_t ws_size,
                              hipStream_t stream) {
  (void)hipFuncSetAttribute((const void*)k1_proj, hipFuncAttributeMaxDynamicSharedMemorySize, 4 * K1_STAGE);
  (void)hipFuncSetAttribute((const void*)k2_attn, hipFuncAttributeMaxDynamicSharedMemorySize, 131072);
  (void)hipFuncSetAttribute((const void*)k3_out, hipFuncAttributeMaxDynamicSharedMemorySize, 147456);
  char* ws = (char*)d_ws;
  k1_proj<<<256, 512, 4 * K1_STAGE, stream>>>((const char*)d_in[0], (const char*)d_in[6], (const char*)d_in[3], (const char*)d_in[4],
                                              (const u16*)d_in[7], (const char*)d_in[8], ws);
  k2_attn<<<256, 512, 131072, stream>>>(ws);
  kg3<<<256, 512, 0, stream>>>((const char*)d_in[1], (const char*)d_in[2], ws);
  k3_out<<<256, 512, 147456, stream>>>(ws, (char*)d_out);
}
```

```cpp
#include <hip/hip_runtime.h>
#include <hip/hip_bf16.h>
#include <stdint.h>

typedef __bf16 bf16x2 __attribute__((ext_vector_type(2)));
typedef __bf16 bf16x8 __attribute__((ext_vector_type(8)));
typedef __bf16 bf16x16 __attribute__((ext_vector_type(16)));
typedef short s16x4 __attribute__((ext_vector_type(4)));
typedef short s16x8 __attribute__((ext_vector_type(8)));
typedef float f32x2 __attribute__((ext_vector_type(2)));
typedef float f32x4 __attribute__((ext_vector_type(4)));
typedef float f32x8 __attribute__((ext_vector_type(8)));
typedef float f32x16 __attribute__((ext_vector_type(16)));
typedef unsigned u32x4 __attribute__((ext_vector_type(4)));
typedef unsigned short u16;
#define DI __device__ __forceinline__
#define LDSP __attribute__((address_space(3)))

constexpr int NP = 2;
constexpr size_t WS_Q = 0, WS_KV = 2u << 20, WS_AB = 6u << 20, WS_F = 10u << 20, WS_G = WS_F + 524288, WS_O = 11u << 20, WS_Y = 14u << 20;
constexpr float QSCALE = 0.25f * 1.4426950408889634f;

DI unsigned pk2(float lo, float hi) { f32x2 v = {lo, hi}; return __builtin_bit_cast(unsigned, __builtin_convertvector(v, bf16x2)); }
DI float bf2f(unsigned short b) { return __builtin_bit_cast(float, (unsigned)b << 16); }
DI float rbf(float x) { return bf2f((u16)(pk2(x, 0.f) & 0xffffu)); }
DI f32x16 mfma(bf16x8 a, bf16x8 b, f32x16 c) { return __builtin_amdgcn_mfma_f32_32x32x16_bf16(a, b, c, 0, 0, 0); }
DI void dma16(unsigned voff, const void* sbase, unsigned ldsaddr) {
  asm volatile("s_mov_b32 m0, %2\n\ts_nop 0\n\tglobal_load_lds_dwordx4 %0, %1" ::"v"(voff), "s"(sbase), "s"(ldsaddr) : "memory");
}
DI void dma16_dev(unsigned voff, const void* sbase, unsigned ldsaddr) {
  asm volatile("s_mov_b32 m0, %2\n\ts_nop 0\n\tglobal_load_lds_dwordx4 %0, %1 sc1" ::"v"(voff), "s"(sbase), "s"(ldsaddr) : "memory");
}
template <int N> DI void wait_bar() { asm volatile("s_waitcnt vmcnt(%0) lgkmcnt(0)\n\ts_barrier" ::"i"(N) : "memory"); }
DI bf16x8 lds_b128(unsigned addr) { return *(const LDSP bf16x8*)(uintptr_t)addr; }
template <int D> DI bf16x8 lds_tr2(unsigned addr) {
  s16x4 lo = __builtin_amdgcn_ds_read_tr16_b64_v4i16((LDSP s16x4*)(uintptr_t)addr);
  s16x4 hi = __builtin_amdgcn_ds_read_tr16_b64_v4i16((LDSP s16x4*)(uintptr_t)(addr + D));
  s16x8 v = __builtin_shufflevector(lo, hi, 0, 1, 2, 3, 4, 5, 6, 7);
  return __builtin_bit_cast(bf16x8, v);
}
#define HZ ""
#define STPOL "sc1"
DI void st16(void* p, u32x4 v) { asm volatile("global_store_dwordx4 %0, %1, off " STPOL "\n\ts_nop 1" ::"v"(p), "v"(v) : "memory"); }
DI void st16s(const void* sbase, unsigned voff, u32x4 v) {
  asm volatile(HZ "global_store_dwordx4 %0, %1, %2 " STPOL "\n\ts_nop 1" ::"v"(voff), "v"(v), "s"(sbase) : "memory");
}
DI u32x4 pack8(const f32x16& a, int o, float s) {
  u32x4 r = {pk2(a[o] * s, a[o + 1] * s), pk2(a[o + 2] * s, a[o + 3] * s), pk2(a[o + 4] * s, a[o + 5] * s), pk2(a[o + 6] * s, a[o + 7] * s)};
  return r;
}
DI u32x4 swap_halves(u32x4 v) {
  auto x = __builtin_amdgcn_permlane32_swap(v[0], v[2], false, false);
  auto y = __builtin_amdgcn_permlane32_swap(v[1], v[3], false, false);
  u32x4 r = {x[0], y[0], x[1], y[1]};
  return r;
}

#define ST_DECL
#define ST(j)
#define ST_FLUSH(k)
DI f32x16 smf(bf16x8 a, const f32x16& p, int o, f32x16 c) {
  f32x8 t = o ? __builtin_shufflevector(p, p, 8, 9, 10, 11, 12, 13, 14, 15) : __builtin_shufflevector(p, p, 0, 1, 2, 3, 4, 5, 6, 7);
  return __builtin_amdgcn_smfmac_f32_32x32x32_bf16(a, __builtin_bit_cast(bf16x16, t), c, 0xdddd, 0, 0);
}
DI float rowmax16(const f32x16& s) {
  float m = s[0];
#pragma unroll
  for (int j = 1; j < 16; ++j) m = fmaxf(m, s[j]);
  float m2 = m;
  asm volatile("s_nop 1\n\tv_permlane32_swap_b32 %0, %1" : "+v"(m), "+v"(m2));
  return fmaxf(m, m2);
}
DI f32x16 splat16(float v) { f32x16 r; for (int j = 0; j < 16; ++j) r[j] = v; return r; }
DI f32x16 attn_true_max(const char* kv, bf16x8 q, int lane) {
  const int r = lane & 31, h = lane >> 5;
  const s16x8 one = {0x3f80, 0x3f80, 0x3f80, 0x3f80, 0x3f80, 0x3f80, 0x3f80, 0x3f80};
  float m = -INFINITY;
#pragma unroll 1
  for (int t = 0; t < 64; ++t) m = fmaxf(m, rowmax16(mfma(*(const bf16x8*)(kv + t * 2048 + lane * 16), q, f32x16{})));
  const f32x16 nm = splat16(-m);
  f32x16 o = {};
#pragma unroll 1
  for (int t = 0; t < 64; ++t) {
    f32x16 s = mfma(*(const bf16x8*)(kv + t * 2048 + lane * 16), q, nm);
    const char* vp = kv + t * 2048 + 1024 + h * 256 + (r & 15) * 16;
    bf16x8 v0 = *(const bf16x8*)vp, v1 = *(const bf16x8*)(vp + 512);
    if (r >= 16) { v0 = __builtin_bit_cast(bf16x8, one); v1 = v0; }
#pragma unroll
    for (int j = 0; j < 16; ++j) s[j] = __builtin_amdgcn_exp2f(s[j]);
    o = smf(v0, s, 0, o); o = smf(v1, s, 1, o);
  }
  return o;
}

#define P1_FRAGS "v48", "v49", "v50", "v51", "v52", "v53", "v54", "v55", "v56", "v57", "v58", "v59", "v60", "v61", "v62", "v63", "v64", "v65", "v66", "v67", "v68", "v69", "v70", "v71", "v72", "v73", "v74", "v75", "v76", "v77", "v78", "v79", "v80", "v81", "v82", "v83", "v84", "v85", "v86", "v87", "v88", "v89", "v90", "v91", "v92", "v93", "v94", "v95"
#define P2_CLOBBER "v0", "v1", "v2", "v3", "v4", "v5", "v6", "v7", "v8", "v9", "v10", "v11", "v12", "v13", "v14", "v15", "v16", "v17", "v18", "v19", "v20", "v21", "v22", "v23", "v96", "v97", "v98", "v99", "v100", "v101", "v102", "v103", "v104", "v105", "v106", "v107", "v108", "v109", "v110", "v111", "v112", "v113", "v114", "v115", "v116", "v117", "v118", "v119", "v120", "v121", "v122", "v123", "v124", "v125", "v126", "v127"
constexpr unsigned LDS_XO = 0, LDS_R = 16384, LDS_XL = 32768, LDS_C = 65536, LDS_WQ = 81920, LDS_WO = 114688, LDS_XB = 16384;
constexpr unsigned LDS_GW = 143360, LDS_JUNK = LDS_GW + 1024, LDS_BYTES = 163840;
#define QPUB 1
#define P1ROT (mt + 2 * nt)
typedef unsigned long long u64;
typedef unsigned u32x2 __attribute__((ext_vector_type(2)));
typedef unsigned u32x8 __attribute__((ext_vector_type(8)));
typedef unsigned u32x16 __attribute__((ext_vector_type(16)));
DI u32x4 srd(const void* p, unsigned bytes) {
  const uint64_t a = (uint64_t)p;
  u32x4 r = {(unsigned)a, (unsigned)(a >> 32) & 0xffffu, bytes, 0x00020000u};
  return r;
}
DI void touch(const char* base, unsigned off, unsigned junk) {
  asm volatile(HZ "s_mov_b32 m0, %2\n\ts_nop 0\n\tglobal_load_lds_dword %0, %1" ::"v"(off), "s"(base), "s"(junk) : "memory");
}
DI int late_block() {
  int b = blockIdx.x;
  asm volatile("" : "+s"(b));
  return __builtin_amdgcn_readfirstlane(b);
}
DI void bar() { asm volatile("s_barrier" ::: "memory"); }
DI void drain_bar() { asm volatile("s_waitcnt vmcnt(0) lgkmcnt(0)\n\ts_barrier" ::: "memory"); }
DI void set_flag(u64* flag, u64 tag) {
  if (threadIdx.x == 0) __hip_atomic_store(flag, tag, __ATOMIC_RELAXED, __HIP_MEMORY_SCOPE_AGENT);
}
DI void await_flags(const u64* flag, u64 tag) {
#pragma unroll 1
  for (int spin = 0; spin < 400000; ++spin) {
    if (__all(__hip_atomic_load(flag, __ATOMIC_RELAXED, __HIP_MEMORY_SCOPE_AGENT) == tag)) break;
    __builtin_amdgcn_s_sleep(1);
  }
}
#define ABPOLL (8 * p + 2 * e4 + (qs >> 1)) * 16 + 8 * k2 + 4 * (qs & 1) + (lane & 3)
#define CLINES 8
#define TLB_WAVE 3
#define YSLEEP "1"
#define FS 8
constexpr int F_Q = 128, F_A = 384, F_O = 640, F_Y = 896;

__global__ __launch_bounds__(512, 2) void fused(const char* x, const char* cre, const char* cim, const char* dre, const char* dim, const char* wqkv,
                                                const char* wo, const char* gate_w, char* ws, char* out) {
  extern __shared__ __attribute__((aligned(16))) char lds[];
  ST_DECL; ST(0);
  const int w = __builtin_amdgcn_readfirstlane(threadIdx.x >> 6), qs = w & 3, g = w >> 2;
#define TILE_INDICES(B)                                                                                   \
  const int b = (B), xcd = b & 7, i = b >> 3;                                                             \
  u64* const fw = (u64*)(ws + WS_F);                                                                      \
  const int mt = 8 * (xcd & 1) + (i & 7), nt = 4 * (xcd >> 1) + (i >> 3), tt1 = 4 * mt + qs;              \
  const int np = xcd >> 1, p = np >> 1, k2 = np & 1, tb = (xcd & 1) * 8 + (i >> 2), e4 = i & 3, eb = 2 * e4 + p; \
  const int head = xcd * 4 + (i >> 3), qb = i & 7, qt = qb * 8 + qs * 2, tb3 = 2 * tb + k2;                     \
  const char* const cb = (p ? cim : cre) + (size_t)tb * 524288 + k2 * 2048;                                        \
  const char* const kv = ws + WS_KV + (size_t)head * 131072;                                              \
  const char* const qp = ws + WS_Q + (size_t)(head * 64 + qt) * 1024;
  const unsigned l0 = (unsigned)(uintptr_t)lds;
  f32x16 g0, g1;
  unsigned late, fwait;
  u64 tag;
#define ST_OUT
#define ST_OUT1
#define ST_OUT2
  const int lane0 = threadIdx.x & 63;
#define P1_READ_ADDRESSES                                                                                        \
  u32x4 ax, ad;                                                                                                   \
  _Pragma("unroll") for (int kk = 0; kk < 4; ++kk) {                                                              \
    const unsigned sw16 = ((2 * kk + h) ^ ((r >> 1) & 7)) * 16;                                                   \
    ax[kk] = l0 + (32 * qs + r) * 128 + sw16;                                                                     \
    ad[kk] = l0 + 16384 + (2 * g + (r >> 4)) * 3072 + (r & 15) * 128 + sw16;                                      \
  }                                                                                                               \
  unsigned aw = l0 + 16384 + (8 * h + ((lane & 15) >> 2)) * 64 + ((lane >> 4) & 1) * 32 + (lane & 3) * 8;
#define P2_READ_ADDRESSES                                                                                                   \
  u32x2 la = {l0 + LDS_R + g * 6144 + lane * 16, l0 + LDS_R + g * 6144 + 5120 + h * 256 + (r & 15) * 16};                     \
  u32x4 lc;                                                                                                                  \
  _Pragma("unroll") for (int kk = 0; kk < 4; ++kk) lc[kk] = l0 + LDS_C + (32 * qs + r) * 128 + (((2 * kk + h) ^ ((r >> 1) & 7)) * 16); \
  unsigned t0 = l0 + LDS_R + 12288 + 4096 + lane * 16, t1 = (r == 16 || r == 20) ? 0x3f803f80u : 0u;                          \
  u32x4 swo = srd(wo + eb * 128, 524288 - eb * 128);                                                                         \
  const unsigned vw0 = (g ? 96 * qs : 384 + 32 * qs) * 1024 + rr * 1024 + ((pc ^ ((rr & 2) << 1)) * 16);                     \
  u32x4 vwo = {vw0, vw0 + 7168, vw0 + 14336, vw0 + 21504};                                                                   \
  unsigned m0wo = g ? l0 + LDS_WO + qs * 12288 : l0 + LDS_WQ + qs * 4096;                                                     \
  fwait = (lane < 8 ? F_O + (tb3 >> 2) * 32 + 8 * (w >> 1) + lane : F_A + (lane & 7) * 16 + (tb3 >> 1)) * 8 * FS;
#define P2_PINS "+{v[204:205]}"(la), "+{v[206:209]}"(lc), "+{v220}"(t0), "+{v221}"(t1), "+{s[84:87]}"(swo), "+{v[232:235]}"(vwo), "+{s83}"(m0wo), "+{v236}"(fwait)
#define P2_STATE u32x8 sz; u32x16 vs, oa, ob; u32x4 k0, kf; unsigned idx, m1, m2;
#define P2_INIT(c) c "{v[24:31]}"(sz), c "{v[140:155]}"(vs), c "{v[156:171]}"(oa), c "{v[172:187]}"(ob), c "{v[188:203]}"(g0), c "{v[238:253]}"(g1), c "{v219}"(idx)
#define P2_FIRST "={v[48:51]}"(k0), "={v[136:139]}"(kf), "+{v[140:155]}"(vs), "+{v[32:47]}"(ae), "={s60}"(m1), "={s61}"(m2)
#define P2_HELD P2_INIT("+"), "+{v[48:51]}"(k0), "+{v[136:139]}"(kf), "+{v[128:135]}"(q), "+{v220}"(t0), "+{v221}"(t1)
#define P2_READS "{v[204:205]}"(la), "{v[206:209]}"(lc)
#define P2_WO "{s[84:87]}"(swo), "{v[232:235]}"(vwo), "{s83}"(m0wo)
  if (g == 0) {
    TILE_INDICES(late_block())
    const int lane = lane0, r = lane & 31, h = lane >> 5, rr = lane >> 3, pc = lane & 7;
    tag = __hip_atomic_load(fw, __ATOMIC_RELAXED, __HIP_MEMORY_SCOPE_AGENT) + 1;
    {
      const char* pcode; unsigned len;
      asm volatile("s_getpc_b64 %0\nfused_code_begin:\n\ts_mov_b32 %1, fused_code_end-fused_code_begin" : "=s"(pcode), "=s"(len));
      const unsigned o = threadIdx.x * 128;
      touch(pcode, o < len ? o : 0, l0 + LDS_JUNK);
    }
    const int pr = (r & 19) | ((r & 4) << 1) | ((r & 8) >> 1);
    const char* kd = ws + WS_KV + (size_t)(2 * nt * 64 + tt1) * 2048;
    const char* qd = ws + WS_Q + (size_t)(2 * nt * 64 + tt1) * 1024;
    unsigned vk = (h * 32 + pr) * 16;
    P2_READ_ADDRESSES
    const int gl = qs >> 1;
    u32x4 sc = srd(cb, 524288 - k2 * 2048), sab = srd(ws + WS_AB - 1024 + (size_t)(p * 16 + 4 * e4) * 131072 + k2 * 65536, 525312 - k2 * 65536);
    u32x4 skv = srd(kv, 131072);
    unsigned m0c = l0 + LDS_C + qs * 4096, m0ab = l0 + LDS_R + gl * 6144 + (qs & 1) * 1024, m0kv = l0 + LDS_R + gl * 6144 + 4096 + (qs & 1) * 1024;
    const unsigned ca = (32 * qs + rr) * 4096 + ((pc ^ (rr >> 1)) * 16), cc = (32 * qs + 8 + rr) * 4096 + ((pc ^ ((rr >> 1) + 4)) * 16) - 1024;
    u32x4 vc = {ca, cc, ca + 65536 - 2048, cc + 65536 - 2048};
    u32x2 vab = {(unsigned)(qs * 131072 + lane * 16 + 1024), (unsigned)(qs * 131072 + lane * 16)};
    unsigned vkv = gl * 65536 + (qs & 1) * 1024 + lane * 16;
    u32x2 xa = {l0 + LDS_XO + qs * 4096 + lane * 16, l0 + LDS_XL + qs * 512 + lane * 4};
    u32x2 fo = {(unsigned)((F_Q + (head >> 1) * 16 + 4 * qs + (lane & 3)) * 8 * FS), (unsigned)((F_Q + nt * 16 + mt) * 8 * FS)};
    const char* ab = ws + WS_AB + ((size_t)((nt >> 3) * 16 + 2 * (nt & 7)) * 128 + 2 * tt1) * 1024;
    unsigned soc = 0, soab = 0, sokv = 0;
    asm volatile("" : "+{s[40:43]}"(sc), "+{s[44:47]}"(sab), "+{s[48:51]}"(skv), "+{s56}"(m0c), "+{s57}"(m0ab), "+{s62}"(m0kv),
                 P2_PINS, "+{v[212:215]}"(vc), "+{v[216:217]}"(vab), "+{v218}"(vkv), "+{v[222:223]}"(xa), "+s"(ab), "+s"(kd), "+s"(qd), "+v"(vk),
                 "+{v[226:227]}"(fo), "+{v[230:231]}"(tag));
    if (w == TLB_WAVE) {
      const char* a = lane < 24   ? ws + (size_t)lane * 1310720
                      : lane < 32 ? cb + (lane - 24) * 65536
                      : lane < 40 ? wo + (lane - 32) * 65536
                      : lane < 41 ? (nt < 8 ? dre : dim) + (size_t)(nt & 7) * 65536
                                  : out + (size_t)tb3 * 65536;
      asm volatile("global_load_lds_dword %0, off sc1" ::"v"(a) : "memory");
    }
    if (w == 1) {
      const unsigned o = lane < 2    ? WS_Q + (2 * nt + lane) * 65536
                         : lane < 6  ? WS_KV + (2 * nt + ((lane - 2) >> 1)) * 131072 + (mt >> 3) * 65536
                         : lane < 8  ? WS_AB + ((nt >> 3) * 16 + 2 * (nt & 7) + (lane & 1)) * 131072 + (mt >> 3) * 65536
                         : lane < 10 ? WS_KV + head * 131072 + (lane & 1) * 65536
                         : lane < 11 ? WS_Q + head * 65536
                         : lane < 12 ? WS_G + (nt >> 3) * 65536
                         : lane < 16 ? WS_AB + (p * 16 + 4 * e4 + (lane & 3)) * 131072 + k2 * 65536
                         : lane < 20 ? WS_O + qb * 262144 + (lane & 3) * 65536
                         : lane < 21 ? WS_O + tb3 * 65536
                         : lane < 22 ? WS_G
                         : lane < 26 ? WS_Y + ((2 * tb + (lane & 1)) * 8 + 2 * e4 + ((lane >> 1) & 1)) * 32768
                                     : WS_F + (lane & 1) * 65536;
      asm volatile(HZ "global_load_lds_dword %0, %1 sc1" ::"v"(o), "s"(ws) : "memory");
    }
    P1_READ_ADDRESSES
    f32x16 a0, a1, ae;
    u32x16 fa, fb, fc;
    asm volatile(
    HZ
    "v_mov_b32 v0, 0\n\t"
    "v_mov_b32 v1, 0\n\t"
    "v_mov_b32 v2, 0\n\t"
    "v_mov_b32 v3, 0\n\t"
    "v_mov_b32 v4, 0\n\t"
    "v_mov_b32 v5, 0\n\t"
    "v_mov_b32 v6, 0\n\t"
    "v_mov_b32 v7, 0\n\t"
    "v_mov_b32 v8, 0\n\t"
    "v_mov_b32 v9, 0\n\t"
    "v_mov_b32 v10, 0\n\t"
    "v_mov_b32 v11, 0\n\t"
    "v_mov_b32 v12, 0\n\t"
    "v_mov_b32 v13, 0\n\t"
    "v_mov_b32 v14, 0\n\t"
    "v_mov_b32 v15, 0\n\t"
    "v_mov_b32 v16, 0\n\t"
    "v_mov_b32 v17, 0\n\t"
    "v_mov_b32 v18, 0\n\t"
    "v_mov_b32 v19, 0\n\t"
    "v_mov_b32 v20, 0\n\t"
    "v_mov_b32 v21, 0\n\t"
    "v_mov_b32 v22, 0\n\t"
    "v_mov_b32 v23, 0\n\t"
    "v_mov_b32 v24, 0\n\t"
    "v_mov_b32 v25, 0\n\t"
    "v_mov_b32 v26, 0\n\t"
    "v_mov_b32 v27, 0\n\t"
    "v_mov_b32 v28, 0\n\t"
    "v_mov_b32 v29, 0\n\t"
    "v_mov_b32 v30, 0\n\t"
    "v_mov_b32 v31, 0\n\t"
    "v_mov_b32 v32, 0\n\t"
    "v_mov_b32 v33, 0\n\t"
    "v_mov_b32 v34, 0\n\t"
    "v_mov_b32 v35, 0\n\t"
    "v_mov_b32 v36, 0\n\t"
    "v_mov_b32 v37, 0\n\t"
    "v_mov_b32 v38, 0\n\t"
    "v_mov_b32 v39, 0\n\t"
    "v_mov_b32 v40, 0\n\t"
    "v_mov_b32 v41, 0\n\t"
    "v_mov_b32 v42, 0\n\t"
    "v_mov_b32 v43, 0\n\t"
    "v_mov_b32 v44, 0\n\t"
    "v_mov_b32 v45, 0\n\t"
    "v_mov_b32 v46, 0\n\t"
    "v_mov_b32 v47, 0\n\t"
    "v_mov_b32 v72, 0\n\t"
    "v_mov_b32 v73, 0\n\t"
    "v_mov_b32 v74, 0\n\t"
    "v_mov_b32 v75, 0\n\t"
    "v_mov_b32 v76, 0\n\t"
    "v_mov_b32 v77, 0\n\t"
    "v_mov_b32 v78, 0\n\t"
    "v_mov_b32 v79, 0\n\t"
    "v_mov_b32 v80, 0\n\t"
    "v_mov_b32 v81, 0\n\t"
    "v_mov_b32 v82, 0\n\t"
    "v_mov_b32 v83, 0\n\t"
    "v_mov_b32 v84, 0\n\t"
    "v_mov_b32 v85, 0\n\t"
    "v_mov_b32 v86, 0\n\t"
    "v_mov_b32 v87, 0\n\t"
    "v_mov_b32 v88, 0\n\t"
    "v_mov_b32 v89, 0\n\t"
    "v_mov_b32 v90, 0\n\t"
    "v_mov_b32 v91, 0\n\t"
    "v_mov_b32 v92, 0\n\t"
    "v_mov_b32 v93, 0\n\t"
    "v_mov_b32 v94, 0\n\t"
    "v_mov_b32 v95, 0\n\t"
    "s_mov_b32 s58, 0\n\t"
    "s_mov_b32 s59, 0\n\t"
    "s_mov_b32 s64, 28672\n\t"
    ".p2align 6\n\t"
    "1:\n\t"
    "s_barrier\n\t"
    "s_waitcnt lgkmcnt(7)\n\t"
    "v_mfma_f32_32x32x16_bf16 v[0:15], v[76:79], v[72:75], v[0:15]\n\t"
    "ds_read_b128 v[48:51], v100\n\t"
    "ds_read_b64_tr_b16 v[52:53], v104 offset:0\n\t"
    "ds_read_b64_tr_b16 v[54:55], v104 offset:256\n\t"
    "s_waitcnt lgkmcnt(8)\n\t"
    "v_mfma_f32_32x32x16_bf16 v[16:31], v[80:83], v[72:75], v[16:31]\n\t"
    "ds_read_b64_tr_b16 v[56:57], v104 offset:1024\n\t"
    "ds_read_b64_tr_b16 v[58:59], v104 offset:1280\n\t"
    "s_add_u32 s59, s59, 28672\n\t"
    "s_cmp_eq_u32 s59, 143360\n\t"
    "s_cselect_b32 s60, 4294852608, s64\n\t"
    "s_cselect_b32 s59, 0, s59\n\t"
    "s_waitcnt lgkmcnt(7)\n\t"
    "v_mfma_f32_32x32x16_bf16 v[0:15], v[88:91], v[84:87], v[0:15]\n\t"
    "ds_read_b128 v[60:63], v101\n\t"
    "ds_read_b64_tr_b16 v[64:65], v104 offset:3072\n\t"
    "ds_read_b64_tr_b16 v[66:67], v104 offset:3328\n\t"
    "s_waitcnt lgkmcnt(8)\n\t"
    "v_mfma_f32_32x32x16_bf16 v[16:31], v[92:95], v[84:87], v[16:31]\n\t"
    "ds_read_b64_tr_b16 v[68:69], v104 offset:4096\n\t"
    "ds_read_b64_tr_b16 v[70:71], v104 offset:4352\n\t"
    "v_add_u32 v100, s60, v100\n\t"
    "v_add_u32 v108, s60, v108\n\t"
    "s_waitcnt lgkmcnt(7)\n\t"
    "v_mfma_f32_32x32x16_bf16 v[0:15], v[52:55], v[48:51], v[0:15]\n\t"
    "ds_read_b128 v[72:75], v102\n\t"
    "ds_read_b64_tr_b16 v[76:77], v104 offset:6144\n\t"
    "ds_read_b64_tr_b16 v[78:79], v104 offset:6400\n\t"
    "s_waitcnt lgkmcnt(8)\n\t"
    "v_mfma_f32_32x32x16_bf16 v[16:31], v[56:59], v[48:51], v[16:31]\n\t"
    "ds_read_b64_tr_b16 v[80:81], v104 offset:7168\n\t"
    "ds_read_b64_tr_b16 v[82:83], v104 offset:7424\n\t"
    "v_add_u32 v101, s60, v101\n\t"
    "v_add_u32 v109, s60, v109\n\t"
    "s_add_u32 s58, s58, 1\n\t"
    "s_waitcnt lgkmcnt(7)\n\t"
    "v_mfma_f32_32x32x16_bf16 v[0:15], v[64:67], v[60:63], v[0:15]\n\t"
    "ds_read_b128 v[84:87], v103\n\t"
    "ds_read_b64_tr_b16 v[88:89], v104 offset:9216\n\t"
    "ds_read_b64_tr_b16 v[90:91], v104 offset:9472\n\t"
    "s_waitcnt lgkmcnt(8)\n\t"
    "v_mfma_f32_32x32x16_bf16 v[16:31], v[68:71], v[60:63], v[16:31]\n\t"
    "ds_read_b64_tr_b16 v[92:93], v104 offset:10240\n\t"
    "ds_read_b64_tr_b16 v[94:95], v104 offset:10496\n\t"
    "v_add_u32 v102, s60, v102\n\t"
    "v_add_u32 v110, s60, v110\n\t"
    "v_add_u32 v103, s60, v103\n\t"
    "v_add_u32 v111, s60, v111\n\t"
    "v_add_u32 v104, s60, v104\n\t"
    "s_cmp_eq_u32 s58, 99\n\t"
    "s_cbranch_scc1 2f\n\t"
    "3:\n\t"
    "s_cmp_lg_u32 s58, 8\n\t"
    "s_cbranch_scc1 1b\n\t"
    "s_barrier\n\t"
    "s_waitcnt lgkmcnt(7)\n\t"
    "v_mfma_f32_32x32x16_bf16 v[0:15], v[76:79], v[72:75], v[0:15]\n\t"
    "ds_read_b128 v[48:51], v100\n\t"
    "ds_read_b128 v[52:55], v108\n\t"
    "s_waitcnt lgkmcnt(7)\n\t"
    "v_mfma_f32_32x32x16_bf16 v[16:31], v[80:83], v[72:75], v[16:31]\n\t"
    "s_waitcnt lgkmcnt(4)\n\t"
    "v_mfma_f32_32x32x16_bf16 v[0:15], v[88:91], v[84:87], v[0:15]\n\t"
    "ds_read_b128 v[60:63], v101\n\t"
    "ds_read_b128 v[64:67], v109\n\t"
    "s_waitcnt lgkmcnt(4)\n\t"
    "v_mfma_f32_32x32x16_bf16 v[16:31], v[92:95], v[84:87], v[16:31]\n\t"
    "v_add_u32 v100, 28672, v100\n\t"
    "v_add_u32 v108, 28672, v108\n\t"
    "s_waitcnt lgkmcnt(2)\n\t"
    "v_mfma_f32_32x32x16_bf16 v[32:47], v[48:51], v[52:55], v[32:47]\n\t"
    "ds_read_b128 v[72:75], v102\n\t"
    "ds_read_b128 v[76:79], v110\n\t"
    "v_add_u32 v101, 28672, v101\n\t"
    "v_add_u32 v109, 28672, v109\n\t"
    "s_waitcnt lgkmcnt(2)\n\t"
    "v_mfma_f32_32x32x16_bf16 v[32:47], v[60:63], v[64:67], v[32:47]\n\t"
    "ds_read_b128 v[84:87], v103\n\t"
    "ds_read_b128 v[88:91], v111\n\t"
    "v_add_u32 v102, 28672, v102\n\t"
    "v_add_u32 v110, 28672, v110\n\t"
    "v_add_u32 v103, 28672, v103\n\t"
    "v_add_u32 v111, 28672, v111\n\t"
    "s_waitcnt lgkmcnt(0)\n\t"
    "s_branch 4f\n\t"
    "2:\n\t"
    "global_load_lds_dword v105, s[40:41]\n\t"
    "s_branch 3b\n\t"
    "4:\n\t"
        : "={v[0:15]}"(a0), "={v[16:31]}"(a1), "={v[32:47]}"(ae), "={v[48:63]}"(fa), "={v[64:79]}"(fb), "={v[80:95]}"(fc), "+{v[100:103]}"(ax),
          "+{v104}"(aw), "+{v[108:111]}"(ad) ST_OUT1
        : "{s[40:43]}"(sc), "{v105}"(0)
        : "memory", "scc", "s58", "s59", "s60", "s64");
    ST(1);
    st16s(kd, vk, pack8(a1, 0, 1.f));
    st16s(kd, vk + 131072, pack8(a1, 8, 1.f));
    st16s(qd, lane * 16, pack8(a0, 0, QSCALE));
    st16s(qd, lane * 16 + 65536, pack8(a0, 8, QSCALE));
    u32x8 q;
    P2_STATE
    asm volatile(
    HZ
    "s_barrier\n\t"
    "s_waitcnt lgkmcnt(2)\n\t"
    "v_mfma_f32_32x32x16_bf16 v[32:47], v[72:75], v[76:79], v[32:47]\n\t"
    "ds_read_b128 v[48:51], v100\n\t"
    "ds_read_b128 v[52:55], v108\n\t"
    "s_waitcnt lgkmcnt(2)\n\t"
    "v_mfma_f32_32x32x16_bf16 v[32:47], v[84:87], v[88:91], v[32:47]\n\t"
    "ds_read_b128 v[60:63], v101\n\t"
    "ds_read_b128 v[64:67], v109\n\t"
    "v_add_u32 v100, 4294852608, v100\n\t"
    "v_add_u32 v108, 4294852608, v108\n\t"
    "s_waitcnt lgkmcnt(2)\n\t"
    "v_mfma_f32_32x32x16_bf16 v[32:47], v[48:51], v[52:55], v[32:47]\n\t"
    "ds_read_b128 v[72:75], v102\n\t"
    "ds_read_b128 v[76:79], v110\n\t"
    "v_add_u32 v101, 4294852608, v101\n\t"
    "v_add_u32 v109, 4294852608, v109\n\t"
    "v_mov_b64 v[24:25], 0\n\t"
    "v_mov_b64 v[26:27], 0\n\t"
    "v_mov_b64 v[28:29], 0\n\t"
    "v_mov_b64 v[30:31], 0\n\t"
    "v_mov_b64 v[156:157], 0\n\t"
    "v_mov_b64 v[158:159], 0\n\t"
    "v_mov_b64 v[160:161], 0\n\t"
    "v_mov_b64 v[162:163], 0\n\t"
    "v_mov_b64 v[164:165], 0\n\t"
    "v_mov_b64 v[166:167], 0\n\t"
    "v_mov_b64 v[168:169], 0\n\t"
    "v_mov_b64 v[170:171], 0\n\t"
    "v_mov_b64 v[172:173], 0\n\t"
    "v_mov_b64 v[174:175], 0\n\t"
    "s_waitcnt lgkmcnt(2)\n\t"
    "v_mfma_f32_32x32x16_bf16 v[32:47], v[60:63], v[64:67], v[32:47]\n\t"
    "ds_read_b128 v[84:87], v103\n\t"
    "ds_read_b128 v[88:91], v111\n\t"
    "v_add_u32 v102, 4294852608, v102\n\t"
    "v_add_u32 v110, 4294852608, v110\n\t"
    "v_add_u32 v103, 4294852608, v103\n\t"
    "v_add_u32 v111, 4294852608, v111\n\t"
    "s_barrier\n\t"
    "s_waitcnt lgkmcnt(2)\n\t"
    "v_mfma_f32_32x32x16_bf16 v[32:47], v[72:75], v[76:79], v[32:47]\n\t"
    "ds_read_b128 v[48:51], v100\n\t"
    "ds_read_b128 v[52:55], v108\n\t"
    "s_waitcnt lgkmcnt(2)\n\t"
    "v_mfma_f32_32x32x16_bf16 v[32:47], v[84:87], v[88:91], v[32:47]\n\t"
    "ds_read_b128 v[60:63], v101\n\t"
    "ds_read_b128 v[64:67], v109\n\t"
    "v_add_u32 v100, 28672, v100\n\t"
    "v_add_u32 v108, 28672, v108\n\t"
    "s_waitcnt lgkmcnt(2)\n\t"
    "v_mfma_f32_32x32x16_bf16 v[32:47], v[48:51], v[52:55], v[32:47]\n\t"
    "ds_read_b128 v[72:75], v102\n\t"
    "ds_read_b128 v[76:79], v110\n\t"
    "v_add_u32 v101, 28672, v101\n\t"
    "v_add_u32 v109, 28672, v109\n\t"
    "v_mov_b64 v[176:177], 0\n\t"
    "v_mov_b64 v[178:179], 0\n\t"
    "v_mov_b64 v[180:181], 0\n\t"
    "v_mov_b64 v[182:183], 0\n\t"
    "v_mov_b64 v[184:185], 0\n\t"
    "v_mov_b64 v[186:187], 0\n\t"
    "v_mov_b64 v[188:189], 0\n\t"
    "v_mov_b64 v[190:191], 0\n\t"
    "v_mov_b64 v[192:193], 0\n\t"
    "v_mov_b64 v[194:195], 0\n\t"
    "v_mov_b64 v[196:197], 0\n\t"
    "v_mov_b64 v[198:199], 0\n\t"
    "v_mov_b64 v[200:201], 0\n\t"
    "v_mov_b64 v[202:203], 0\n\t"
    "s_waitcnt lgkmcnt(2)\n\t"
    "v_mfma_f32_32x32x16_bf16 v[32:47], v[60:63], v[64:67], v[32:47]\n\t"
    "ds_read_b128 v[84:87], v103\n\t"
    "ds_read_b128 v[88:91], v111\n\t"
    "v_add_u32 v102, 28672, v102\n\t"
    "v_add_u32 v110, 28672, v110\n\t"
    "v_add_u32 v103, 28672, v103\n\t"
    "v_add_u32 v111, 28672, v111\n\t"
    "s_waitcnt vmcnt(0)\n\t"
    "s_barrier\n\t"
    "s_cmp_eq_u32 s55, 0\n\t"
    "s_cbranch_scc1 8f\n\t"
    "9:\n\t"
    "s_waitcnt lgkmcnt(2)\n\t"
    "v_mfma_f32_32x32x16_bf16 v[32:47], v[72:75], v[76:79], v[32:47]\n\t"
    "ds_read_b128 v[48:51], v100\n\t"
    "ds_read_b128 v[52:55], v108\n\t"
    "s_waitcnt lgkmcnt(2)\n\t"
    "v_mfma_f32_32x32x16_bf16 v[32:47], v[84:87], v[88:91], v[32:47]\n\t"
    "ds_read_b128 v[60:63], v101\n\t"
    "ds_read_b128 v[64:67], v109\n\t"
    "v_add_u32 v100, 28672, v100\n\t"
    "v_add_u32 v108, 28672, v108\n\t"
    "s_waitcnt lgkmcnt(2)\n\t"
    "v_mfma_f32_32x32x16_bf16 v[32:47], v[48:51], v[52:55], v[32:47]\n\t"
    "ds_read_b128 v[72:75], v102\n\t"
    "ds_read_b128 v[76:79], v110\n\t"
    "v_add_u32 v101, 28672, v101\n\t"
    "v_add_u32 v109, 28672, v109\n\t"
    "v_mov_b64 v[238:239], 0\n\t"
    "v_mov_b64 v[240:241], 0\n\t"
    "v_mov_b64 v[242:243], 0\n\t"
    "v_mov_b64 v[244:245], 0\n\t"
    "v_mov_b64 v[246:247], 0\n\t"
    "v_mov_b64 v[248:249], 0\n\t"
    "v_mov_b64 v[250:251], 0\n\t"
    "v_mov_b64 v[252:253], 0\n\t"
    "v_mov_b32 v140, v221\n\t"
    "v_mov_b32 v141, v221\n\t"
    "v_mov_b32 v142, v221\n\t"
    "v_mov_b32 v143, v221\n\t"
    "v_mov_b32 v144, v221\n\t"
    "v_mov_b32 v145, v221\n\t"
    "s_waitcnt lgkmcnt(2)\n\t"
    "v_mfma_f32_32x32x16_bf16 v[32:47], v[60:63], v[64:67], v[32:47]\n\t"
    "ds_read_b128 v[84:87], v103\n\t"
    "ds_read_b128 v[88:91], v111\n\t"
    "v_add_u32 v102, 28672, v102\n\t"
    "v_add_u32 v110, 28672, v110\n\t"
    "v_add_u32 v103, 28672, v103\n\t"
    "v_add_u32 v111, 28672, v111\n\t"
    "s_barrier\n\t"
    "s_waitcnt lgkmcnt(2)\n\t"
    "v_mfma_f32_32x32x16_bf16 v[32:47], v[72:75], v[76:79], v[32:47]\n\t"
    "ds_read_b128 v[48:51], v100\n\t"
    "ds_read_b128 v[52:55], v108\n\t"
    "s_waitcnt lgkmcnt(2)\n\t"
    "v_mfma_f32_32x32x16_bf16 v[32:47], v[84:87], v[88:91], v[32:47]\n\t"
    "ds_read_b128 v[60:63], v101\n\t"
    "ds_read_b128 v[64:67], v109\n\t"
    "v_add_u32 v100, 28672, v100\n\t"
    "v_add_u32 v108, 28672, v108\n\t"
    "s_waitcnt lgkmcnt(2)\n\t"
    "v_mfma_f32_32x32x16_bf16 v[32:47], v[48:51], v[52:55], v[32:47]\n\t"
    "ds_read_b128 v[72:75], v102\n\t"
    "ds_read_b128 v[76:79], v110\n\t"
    "v_add_u32 v101, 28672, v101\n\t"
    "v_add_u32 v109, 28672, v109\n\t"
    "v_mov_b32 v146, v221\n\t"
    "v_mov_b32 v147, v221\n\t"
    "v_mov_b32 v148, v221\n\t"
    "v_mov_b32 v149, v221\n\t"
    "v_mov_b32 v150, v221\n\t"
    "v_mov_b32 v151, v221\n\t"
    "v_mov_b32 v152, v221\n\t"
    "v_mov_b32 v153, v221\n\t"
    "v_mov_b32 v154, v221\n\t"
    "v_mov_b32 v155, v221\n\t"
    "v_mov_b32 v219, 0xdddd\n\t"
    "s_waitcnt lgkmcnt(2)\n\t"
    "v_mfma_f32_32x32x16_bf16 v[32:47], v[60:63], v[64:67], v[32:47]\n\t"
    "ds_read_b128 v[84:87], v103\n\t"
    "ds_read_b128 v[88:91], v111\n\t"
    "v_add_u32 v102, 28672, v102\n\t"
    "v_add_u32 v110, 28672, v110\n\t"
    "v_add_u32 v103, 28672, v103\n\t"
    "v_add_u32 v111, 28672, v111\n\t"
    "s_barrier\n\t"
    "global_load_dwordx2 v[228:229], v226, s[72:73] sc1\n\t"
    "s_waitcnt lgkmcnt(2)\n\t"
    "v_mfma_f32_32x32x16_bf16 v[32:47], v[72:75], v[76:79], v[32:47]\n\t"
    "ds_read_b128 v[48:51], v100\n\t"
    "ds_read_b128 v[52:55], v108\n\t"
    "s_waitcnt lgkmcnt(2)\n\t"
    "v_mfma_f32_32x32x16_bf16 v[32:47], v[84:87], v[88:91], v[32:47]\n\t"
    "ds_read_b128 v[60:63], v101\n\t"
    "ds_read_b128 v[64:67], v109\n\t"
    "v_add_u32 v100, 28672, v100\n\t"
    "v_add_u32 v108, 28672, v108\n\t"
    "s_waitcnt lgkmcnt(2)\n\t"
    "v_mfma_f32_32x32x16_bf16 v[32:47], v[48:51], v[52:55], v[32:47]\n\t"
    "ds_read_b128 v[72:75], v102\n\t"
    "ds_read_b128 v[76:79], v110\n\t"
    "v_add_u32 v101, 28672, v101\n\t"
    "v_add_u32 v109, 28672, v109\n\t"
    "s_waitcnt lgkmcnt(2)\n\t"
    "v_mfma_f32_32x32x16_bf16 v[32:47], v[60:63], v[64:67], v[32:47]\n\t"
    "ds_read_b128 v[84:87], v103\n\t"
    "ds_read_b128 v[88:91], v111\n\t"
    "v_add_u32 v102, 28672, v102\n\t"
    "v_add_u32 v110, 28672, v110\n\t"
    "v_add_u32 v103, 28672, v103\n\t"
    "v_add_u32 v111, 28672, v111\n\t"
    "s_waitcnt vmcnt(0)\n\t"
    "v_cmp_ne_u64 vcc, v[228:229], v[230:231]\n\t"
    "s_nop 4\n\t"
    "s_cbranch_vccnz 12f\n\t"
    "13:\n\t"
    "s_barrier\n\t"
    "global_load_dwordx4 v[128:131], v125, s[78:79] sc1\n\t"
    "global_load_dwordx4 v[132:135], v125, s[78:79] offset:1024 sc1\n\t"
    "s_add_u32 m0, s62, 12288\n\t"
    "s_nop 0\n\t"
    "buffer_load_dwordx4 v218, s[48:51], s54 offen sc1 lds\n\t"
    "s_add_u32 s54, s54, 2048\n\t"
    "s_add_u32 m0, s62, 24576\n\t"
    "s_nop 0\n\t"
    "buffer_load_dwordx4 v218, s[48:51], s54 offen sc1 lds\n\t"
    "s_add_u32 s54, s54, 2048\n\t"
    "s_waitcnt lgkmcnt(2)\n\t"
    "v_mfma_f32_32x32x16_bf16 v[32:47], v[72:75], v[76:79], v[32:47]\n\t"
    "ds_read_b128 v[48:51], v100\n\t"
    "ds_read_b128 v[52:55], v108\n\t"
    "s_waitcnt lgkmcnt(2)\n\t"
    "v_mfma_f32_32x32x16_bf16 v[32:47], v[84:87], v[88:91], v[32:47]\n\t"
    "ds_read_b128 v[60:63], v101\n\t"
    "ds_read_b128 v[64:67], v109\n\t"
    "v_add_u32 v100, 4294852608, v100\n\t"
    "v_add_u32 v108, 4294852608, v108\n\t"
    "s_waitcnt lgkmcnt(2)\n\t"
    "v_mfma_f32_32x32x16_bf16 v[32:47], v[48:51], v[52:55], v[32:47]\n\t"
    "ds_read_b128 v[72:75], v102\n\t"
    "ds_read_b128 v[76:79], v110\n\t"
    "v_add_u32 v101, 4294852608, v101\n\t"
    "v_add_u32 v109, 4294852608, v109\n\t"
    "s_waitcnt lgkmcnt(2)\n\t"
    "v_mfma_f32_32x32x16_bf16 v[32:47], v[60:63], v[64:67], v[32:47]\n\t"
    "ds_read_b128 v[84:87], v103\n\t"
    "ds_read_b128 v[88:91], v111\n\t"
    "v_add_u32 v102, 4294852608, v102\n\t"
    "v_add_u32 v110, 4294852608, v110\n\t"
    "v_add_u32 v103, 4294852608, v103\n\t"
    "v_add_u32 v111, 4294852608, v111\n\t"
    "s_barrier\n\t"
    "s_waitcnt lgkmcnt(2)\n\t"
    "v_mfma_f32_32x32x16_bf16 v[32:47], v[72:75], v[76:79], v[32:47]\n\t"
    "ds_read_b128 v[48:51], v100\n\t"
    "ds_read_b128 v[52:55], v108\n\t"
    "s_waitcnt lgkmcnt(2)\n\t"
    "v_mfma_f32_32x32x16_bf16 v[32:47], v[84:87], v[88:91], v[32:47]\n\t"
    "ds_read_b128 v[60:63], v101\n\t"
    "ds_read_b128 v[64:67], v109\n\t"
    "v_add_u32 v100, 28672, v100\n\t"
    "v_add_u32 v108, 28672, v108\n\t"
    "s_waitcnt lgkmcnt(2)\n\t"
    "v_mfma_f32_32x32x16_bf16 v[32:47], v[48:51], v[52:55], v[32:47]\n\t"
    "ds_read_b128 v[72:75], v102\n\t"
    "ds_read_b128 v[76:79], v110\n\t"
    "v_add_u32 v101, 28672, v101\n\t"
    "v_add_u32 v109, 28672, v109\n\t"
    "s_waitcnt lgkmcnt(2)\n\t"
    "v_mfma_f32_32x32x16_bf16 v[32:47], v[60:63], v[64:67], v[32:47]\n\t"
    "ds_read_b128 v[84:87], v103\n\t"
    "ds_read_b128 v[88:91], v111\n\t"
    "v_add_u32 v102, 28672, v102\n\t"
    "v_add_u32 v110, 28672, v110\n\t"
    "v_add_u32 v103, 28672, v103\n\t"
    "v_add_u32 v111, 28672, v111\n\t"
    "s_waitcnt lgkmcnt(2)\n\t"
    "v_mfma_f32_32x32x16_bf16 v[32:47], v[72:75], v[76:79], v[32:47]\n\t"
    "s_waitcnt lgkmcnt(0)\n\t"
    "v_mfma_f32_32x32x16_bf16 v[32:47], v[84:87], v[88:91], v[32:47]\n\t"
    "s_waitcnt vmcnt(1)\n\t"
    "s_barrier\n\t"
    "s_branch 14f\n\t"
    "12:\n\t"
    "s_mov_b32 s58, 400000\n\t"
    "11:\n\t"
    "s_sleep 1\n\t"
    "global_load_dwordx2 v[228:229], v226, s[72:73] sc1\n\t"
    "s_waitcnt vmcnt(0)\n\t"
    "v_cmp_ne_u64 vcc, v[228:229], v[230:231]\n\t"
    "s_sub_u32 s58, s58, 1\n\t"
    "s_cbranch_scc1 13b\n\t"
    "s_nop 2\n\t"
    "s_cbranch_vccnz 11b\n\t"
    "s_branch 13b\n\t"
    "8:\n\t"
    "s_mov_b64 exec, 1\n\t"
    "global_store_dwordx2 v227, v[230:231], s[72:73] sc1\n\t"
    "s_mov_b64 exec, -1\n\t"
    "s_branch 9b\n\t"
    "14:\n\t"
        : "+{v[32:47]}"(ae), "+{v[48:63]}"(fa), "+{v[64:79]}"(fb), "+{v[80:95]}"(fc), "+{v[100:103]}"(ax), "+{v[108:111]}"(ad),
          "={v[128:135]}"(q), "+{s54}"(sokv), P2_INIT("=")
        : "{v221}"(t1), "{s55}"(w), "{s[48:51]}"(skv), "{s62}"(m0kv), "{v218}"(vkv), "{s[72:73]}"(fw), "{s[78:79]}"(qp), "{v125}"(lane * 16), "{v[226:227]}"(fo), "{v[230:231]}"(tag)
        : "memory", "scc", "vcc", "s58", "s60", "v228", "v229");
    ST(5);
    asm volatile(
    HZ
    "s_mov_b32 s60, 0xffff\n\t"
    "s_mov_b32 s61, 0xffff\n\t"
    "ds_read_b128 v[48:51], v220 offset:0\n\t"
    "ds_read_b128 v[136:139], v204 offset:16384\n\t"
    "s_mov_b64 exec, s[60:61]\n\t"
    "ds_read_b128 v[140:143], v205 offset:12288\n\t"
    "ds_read_b128 v[144:147], v205 offset:12800\n\t"
    "s_mov_b64 exec, -1\n\t"
    "s_add_u32 m0, s58, 36864\n\t"
    "s_nop 0\n\t"
    "buffer_load_dwordx4 v218, s[48:51], s54 offen sc1 lds\n\t"
    "s_add_u32 s54, s54, 2048\n\t"
    "s_add_u32 m0, s58, 0\n\t"
    "s_nop 0\n\t"
    "buffer_load_dwordx4 v218, s[48:51], s54 offen sc1 lds\n\t"
    "s_add_u32 s54, s54, 2048\n\t"
    "s_nop 3\n\t"
        : "+{s54}"(sokv), P2_FIRST : "{s[48:51]}"(skv), "{s58}"(m0kv), "{v218}"(vkv), "{v220}"(t0), P2_READS : "memory", "scc");
    st16s(ab, lane * 16, swap_halves(pack8(ae, 0, 1.f)));
    st16s(ab, lane * 16 + 1024, swap_halves(pack8(ae, 8, 1.f)));
    asm volatile(
    HZ
    "s_waitcnt lgkmcnt(3)\n\t"
    "v_mfma_f32_32x32x16_bf16 v[0:15], v[48:51], v[128:131], 0\n\t"
    "v_mfma_f32_32x32x16_bf16 v[112:127], v[48:51], v[132:135], 0\n\t"
    "s_nop 11\n\t"
    "v_max3_f32 v220, v0, v1, v2\n\t"
    "v_max3_f32 v221, v3, v4, v5\n\t"
    "v_max3_f32 v220, v220, v6, v7\n\t"
    "v_max3_f32 v221, v221, v8, v9\n\t"
    "v_max3_f32 v220, v220, v10, v11\n\t"
    "v_max3_f32 v221, v221, v12, v13\n\t"
    "v_max3_f32 v220, v220, v14, v15\n\t"
    "v_max_f32 v220, v220, v221\n\t"
    "v_mov_b32 v221, v220\n\t"
    "s_nop 1\n\t"
    "v_permlane32_swap_b32 v220, v221\n\t"
    "s_nop 1\n\t"
    "v_max_f32 v220, v220, v221\n\t"
    "v_sub_f32 v221, 0, v220\n\t"
    "v_sub_f32 v220, 0, v220\n\t"
    "v_mov_b64 v[96:97], v[220:221]\n\t"
    "v_mov_b64 v[98:99], v[220:221]\n\t"
    "v_mov_b64 v[100:101], v[220:221]\n\t"
    "v_mov_b64 v[102:103], v[220:221]\n\t"
    "v_mov_b64 v[104:105], v[220:221]\n\t"
    "v_mov_b64 v[106:107], v[220:221]\n\t"
    "v_mov_b64 v[108:109], v[220:221]\n\t"
    "v_mov_b64 v[110:111], v[220:221]\n\t"
    "s_waitcnt lgkmcnt(2)\n\t"
    "s_nop 0\n\t"
    "v_mfma_f32_32x32x16_bf16 v[0:15], v[136:139], v[128:131], v[96:111]\n\t"
    "v_max3_f32 v220, v112, v113, v114\n\t"
    "v_max3_f32 v221, v115, v116, v117\n\t"
    "v_max3_f32 v220, v220, v118, v119\n\t"
    "v_max3_f32 v221, v221, v120, v121\n\t"
    "v_max3_f32 v220, v220, v122, v123\n\t"
    "v_max3_f32 v221, v221, v124, v125\n\t"
    "v_max3_f32 v220, v220, v126, v127\n\t"
    "v_max_f32 v220, v220, v221\n\t"
    "v_mov_b32 v221, v220\n\t"
    "s_nop 1\n\t"
    "v_permlane32_swap_b32 v220, v221\n\t"
    "s_nop 1\n\t"
    "v_max_f32 v220, v220, v221\n\t"
    "v_sub_f32 v221, 0, v220\n\t"
    "v_sub_f32 v220, 0, v220\n\t"
    "v_mov_b64 v[112:113], v[220:221]\n\t"
    "v_mov_b64 v[114:115], v[220:221]\n\t"
    "v_mov_b64 v[116:117], v[220:221]\n\t"
    "v_mov_b64 v[118:119], v[220:221]\n\t"
    "v_mov_b64 v[120:121], v[220:221]\n\t"
    "v_mov_b64 v[122:123], v[220:221]\n\t"
    "v_mov_b64 v[124:125], v[220:221]\n\t"
    "v_mov_b64 v[126:127], v[220:221]\n\t"
    "s_waitcnt vmcnt(4) lgkmcnt(0)\n\t"
    "s_setprio 1\n\t"
    "s_barrier\n\t"
    "v_exp_f32 v0, v0\n\t"
    "v_exp_f32 v1, v1\n\t"
    "v_smfmac_f32_32x32x32_bf16 v[172:187], v[152:155], v[24:31], v219\n\t"
    "s_add_u32 m0, s58, 12288\n\t"
    "v_exp_f32 v2, v2\n\t"
    "buffer_load_dwordx4 v218, s[48:51], s54 offen sc1 lds\n\t"
    "s_add_u32 s54, s54, 2048\n\t"
    "v_exp_f32 v3, v3\n\t"
    "s_mov_b64 exec, s[60:61]\n\t"
    "ds_read_b128 v[148:151], v205 offset:24576\n\t"
    "ds_read_b128 v[152:155], v205 offset:25088\n\t"
    "s_mov_b64 exec, -1\n\t"
    "v_exp_f32 v4, v4\n\t"
    "v_mfma_f32_32x32x16_bf16 v[16:31], v[136:139], v[132:135], v[112:127]\n\t"
    "v_exp_f32 v5, v5\n\t"
    "v_exp_f32 v6, v6\n\t"
    "ds_read_b128 v[136:139], v204 offset:28672\n\t"
    "v_exp_f32 v7, v7\n\t"
    "v_exp_f32 v8, v8\n\t"
    "v_exp_f32 v9, v9\n\t"
    "v_exp_f32 v10, v10\n\t"
    "v_smfmac_f32_32x32x32_bf16 v[156:171], v[140:143], v[0:7], v219\n\t"
    "v_exp_f32 v11, v11\n\t"
    "v_exp_f32 v12, v12\n\t"
    "v_exp_f32 v13, v13\n\t"
    "v_exp_f32 v14, v14\n\t"
    "v_exp_f32 v15, v15\n\t"
    "v_exp_f32 v16, v16\n\t"
    "v_exp_f32 v17, v17\n\t"
    "v_smfmac_f32_32x32x32_bf16 v[156:171], v[144:147], v[8:15], v219\n\t"
    "v_exp_f32 v18, v18\n\t"
    "v_exp_f32 v19, v19\n\t"
    "s_waitcnt lgkmcnt(0)\n\t"
    "v_exp_f32 v20, v20\n\t"
    "v_mfma_f32_32x32x16_bf16 v[0:15], v[136:139], v[128:131], v[96:111]\n\t"
    "v_exp_f32 v21, v21\n\t"
    "v_exp_f32 v22, v22\n\t"
    "v_exp_f32 v23, v23\n\t"
    "v_exp_f32 v24, v24\n\t"
    "v_exp_f32 v25, v25\n\t"
    "v_exp_f32 v26, v26\n\t"
    "v_smfmac_f32_32x32x32_bf16 v[172:187], v[140:143], v[16:23], v219\n\t"
    "v_exp_f32 v27, v27\n\t"
    "v_exp_f32 v28, v28\n\t"
    "v_exp_f32 v29, v29\n\t"
    "v_exp_f32 v30, v30\n\t"
    "v_exp_f32 v31, v31\n\t"
    "s_waitcnt vmcnt(4) lgkmcnt(0)\n\t"
    "s_barrier\n\t"
    "v_exp_f32 v0, v0\n\t"
    "v_exp_f32 v1, v1\n\t"
    "v_smfmac_f32_32x32x32_bf16 v[172:187], v[144:147], v[24:31], v219\n\t"
    "s_add_u32 m0, s58, 24576\n\t"
    "v_exp_f32 v2, v2\n\t"
    "buffer_load_dwordx4 v218, s[48:51], s54 offen sc1 lds\n\t"
    "s_add_u32 s54, s54, 2048\n\t"
    "v_exp_f32 v3, v3\n\t"
    "s_mov_b64 exec, s[60:61]\n\t"
    "ds_read_b128 v[140:143], v205 offset:36864\n\t"
    "ds_read_b128 v[144:147], v205 offset:37376\n\t"
    "s_mov_b64 exec, -1\n\t"
    "v_exp_f32 v4, v4\n\t"
    "v_mfma_f32_32x32x16_bf16 v[16:31], v[136:139], v[132:135], v[112:127]\n\t"
    "v_exp_f32 v5, v5\n\t"
    "v_exp_f32 v6, v6\n\t"
    "ds_read_b128 v[136:139], v204 offset:40960\n\t"
    "v_exp_f32 v7, v7\n\t"
    "v_exp_f32 v8, v8\n\t"
    "v_exp_f32 v9, v9\n\t"
    "v_exp_f32 v10, v10\n\t"
    "v_smfmac_f32_32x32x32_bf16 v[156:171], v[148:151], v[0:7], v219\n\t"
    "v_exp_f32 v11, v11\n\t"
    "v_exp_f32 v12, v12\n\t"
    "v_exp_f32 v13, v13\n\t"
    "v_exp_f32 v14, v14\n\t"
    "v_exp_f32 v15, v15\n\t"
    "v_exp_f32 v16, v16\n\t"
    "v_exp_f32 v17, v17\n\t"
    "v_smfmac_f32_32x32x32_bf16 v[156:171], v[152:155], v[8:15], v219\n\t"
    "v_exp_f32 v18, v18\n\t"
    "v_exp_f32 v19, v19\n\t"
    "s_waitcnt lgkmcnt(0)\n\t"
    "v_exp_f32 v20, v20\n\t"
    "v_mfma_f32_32x32x16_bf16 v[0:15], v[136:139], v[128:131], v[96:111]\n\t"
    "v_exp_f32 v21, v21\n\t"
    "v_exp_f32 v22, v22\n\t"
    "v_exp_f32 v23, v23\n\t"
    "v_exp_f32 v24, v24\n\t"
    "v_exp_f32 v25, v25\n\t"
    "v_exp_f32 v26, v26\n\t"
    "v_smfmac_f32_32x32x32_bf16 v[172:187], v[148:151], v[16:23], v219\n\t"
    "v_exp_f32 v27, v27\n\t"
    "v_exp_f32 v28, v28\n\t"
    "v_exp_f32 v29, v29\n\t"
    "v_exp_f32 v30, v30\n\t"
    "v_exp_f32 v31, v31\n\t"
    "s_waitcnt vmcnt(2) lgkmcnt(0)\n\t"
    "s_barrier\n\t"
    "v_exp_f32 v0, v0\n\t"
    "v_exp_f32 v1, v1\n\t"
    "v_smfmac_f32_32x32x32_bf16 v[172:187], v[152:155], v[24:31], v219\n\t"
    "s_add_u32 m0, s58, 36864\n\t"
    "v_exp_f32 v2, v2\n\t"
    "buffer_load_dwordx4 v218, s[48:51], s54 offen sc1 lds\n\t"
    "s_add_u32 s54, s54, 2048\n\t"
    "v_exp_f32 v3, v3\n\t"
    "s_mov_b64 exec, s[60:61]\n\t"
    "ds_read_b128 v[148:151], v205 offset:0\n\t"
    "ds_read_b128 v[152:155], v205 offset:512\n\t"
    "s_mov_b64 exec, -1\n\t"
    "v_exp_f32 v4, v4\n\t"
    "v_mfma_f32_32x32x16_bf16 v[16:31], v[136:139], v[132:135], v[112:127]\n\t"
    "v_exp_f32 v5, v5\n\t"
    "v_exp_f32 v6, v6\n\t"
    "ds_read_b128 v[136:139], v204 offset:4096\n\t"
    "v_exp_f32 v7, v7\n\t"
    "v_exp_f32 v8, v8\n\t"
    "v_exp_f32 v9, v9\n\t"
    "v_exp_f32 v10, v10\n\t"
    "v_smfmac_f32_32x32x32_bf16 v[156:171], v[140:143], v[0:7], v219\n\t"
    "v_exp_f32 v11, v11\n\t"
    "v_exp_f32 v12, v12\n\t"
    "v_exp_f32 v13, v13\n\t"
    "v_exp_f32 v14, v14\n\t"
    "v_exp_f32 v15, v15\n\t"
    "v_exp_f32 v16, v16\n\t"
    "v_exp_f32 v17, v17\n\t"
    "v_smfmac_f32_32x32x32_bf16 v[156:171], v[144:147], v[8:15], v219\n\t"
    "v_exp_f32 v18, v18\n\t"
    "v_exp_f32 v19, v19\n\t"
    "s_waitcnt lgkmcnt(0)\n\t"
    "v_exp_f32 v20, v20\n\t"
    "v_mfma_f32_32x32x16_bf16 v[0:15], v[136:139], v[128:131], v[96:111]\n\t"
    "v_exp_f32 v21, v21\n\t"
    "v_exp_f32 v22, v22\n\t"
    "v_exp_f32 v23, v23\n\t"
    "v_exp_f32 v24, v24\n\t"
    "v_exp_f32 v25, v25\n\t"
    "v_exp_f32 v26, v26\n\t"
    "v_smfmac_f32_32x32x32_bf16 v[172:187], v[140:143], v[16:23], v219\n\t"
    "v_exp_f32 v27, v27\n\t"
    "v_exp_f32 v28, v28\n\t"
    "v_exp_f32 v29, v29\n\t"
    "v_exp_f32 v30, v30\n\t"
    "v_exp_f32 v31, v31\n\t"
    "s_waitcnt vmcnt(2) lgkmcnt(0)\n\t"
    "s_barrier\n\t"
    "v_exp_f32 v0, v0\n\t"
    "v_exp_f32 v1, v1\n\t"
    "v_smfmac_f32_32x32x32_bf16 v[172:187], v[144:147], v[24:31], v219\n\t"
    "s_add_u32 m0, s56, 0\n\t"
    "v_exp_f32 v2, v2\n\t"
    "buffer_load_dwordx4 v212, s[40:43], s52 offen offset:0 nt lds\n\t"
    "v_exp_f32 v3, v3\n\t"
    "s_mov_b64 exec, s[60:61]\n\t"
    "ds_read_b128 v[140:143], v205 offset:12288\n\t"
    "ds_read_b128 v[144:147], v205 offset:12800\n\t"
    "s_mov_b64 exec, -1\n\t"
    "v_exp_f32 v4, v4\n\t"
    "v_mfma_f32_32x32x16_bf16 v[16:31], v[136:139], v[132:135], v[112:127]\n\t"
    "v_exp_f32 v5, v5\n\t"
    "v_exp_f32 v6, v6\n\t"
    "ds_read_b128 v[136:139], v204 offset:16384\n\t"
    "v_exp_f32 v7, v7\n\t"
    "v_exp_f32 v8, v8\n\t"
    "buffer_load_dwordx4 v213, s[40:43], s52 offen offset:1024 nt lds\n\t"
    "v_exp_f32 v9, v9\n\t"
    "v_exp_f32 v10, v10\n\t"
    "v_smfmac_f32_32x32x32_bf16 v[156:171], v[148:151], v[0:7], v219\n\t"
    "v_exp_f32 v11, v11\n\t"
    "s_add_u32 m0, s58, 0\n\t"
    "v_exp_f32 v12, v12\n\t"
    "buffer_load_dwordx4 v218, s[48:51], s54 offen sc1 lds\n\t"
    "s_add_u32 s54, s54, 2048\n\t"
    "v_exp_f32 v13, v13\n\t"
    "v_exp_f32 v14, v14\n\t"
    "v_exp_f32 v15, v15\n\t"
    "v_exp_f32 v16, v16\n\t"
    "v_exp_f32 v17, v17\n\t"
    "v_smfmac_f32_32x32x32_bf16 v[156:171], v[152:155], v[8:15], v219\n\t"
    "v_exp_f32 v18, v18\n\t"
    "v_exp_f32 v19, v19\n\t"
    "s_waitcnt lgkmcnt(0)\n\t"
    "v_exp_f32 v20, v20\n\t"
    "v_mfma_f32_32x32x16_bf16 v[0:15], v[136:139], v[128:131], v[96:111]\n\t"
    "v_exp_f32 v21, v21\n\t"
    "v_exp_f32 v22, v22\n\t"
    "v_exp_f32 v23, v23\n\t"
    "v_exp_f32 v24, v24\n\t"
    "v_exp_f32 v25, v25\n\t"
    "v_exp_f32 v26, v26\n\t"
    "v_smfmac_f32_32x32x32_bf16 v[172:187], v[148:151], v[16:23], v219\n\t"
    "v_exp_f32 v27, v27\n\t"
    "v_exp_f32 v28, v28\n\t"
    "v_exp_f32 v29, v29\n\t"
    "v_exp_f32 v30, v30\n\t"
    "v_exp_f32 v31, v31\n\t"
    "s_waitcnt vmcnt(4) lgkmcnt(0)\n\t"
    "s_barrier\n\t"
    "v_exp_f32 v0, v0\n\t"
    "v_exp_f32 v1, v1\n\t"
    "v_smfmac_f32_32x32x32_bf16 v[172:187], v[152:155], v[24:31], v219\n\t"
    "s_add_u32 m0, s56, 0\n\t"
    "v_exp_f32 v2, v2\n\t"
    "buffer_load_dwordx4 v214, s[40:43], s52 offen offset:2048 nt lds\n\t"
    "v_exp_f32 v3, v3\n\t"
    "s_mov_b64 exec, s[60:61]\n\t"
    "ds_read_b128 v[148:151], v205 offset:24576\n\t"
    "ds_read_b128 v[152:155], v205 offset:25088\n\t"
    "s_mov_b64 exec, -1\n\t"
    "v_exp_f32 v4, v4\n\t"
    "v_mfma_f32_32x32x16_bf16 v[16:31], v[136:139], v[132:135], v[112:127]\n\t"
    "v_exp_f32 v5, v5\n\t"
    "v_exp_f32 v6, v6\n\t"
    "ds_read_b128 v[136:139], v204 offset:28672\n\t"
    "v_exp_f32 v7, v7\n\t"
    "v_exp_f32 v8, v8\n\t"
    "buffer_load_dwordx4 v215, s[40:43], s52 offen offset:3072 nt lds\n\t"
    "s_add_u32 s52, s52, 128\n\t"
    "v_exp_f32 v9, v9\n\t"
    "v_exp_f32 v10, v10\n\t"
    "v_smfmac_f32_32x32x32_bf16 v[156:171], v[140:143], v[0:7], v219\n\t"
    "v_exp_f32 v11, v11\n\t"
    "s_add_u32 m0, s58, 12288\n\t"
    "v_exp_f32 v12, v12\n\t"
    "buffer_load_dwordx4 v218, s[48:51], s54 offen sc1 lds\n\t"
    "s_add_u32 s54, s54, 2048\n\t"
    "v_exp_f32 v13, v13\n\t"
    "v_exp_f32 v14, v14\n\t"
    "v_exp_f32 v15, v15\n\t"
    "v_exp_f32 v16, v16\n\t"
    "v_exp_f32 v17, v17\n\t"
    "v_smfmac_f32_32x32x32_bf16 v[156:171], v[144:147], v[8:15], v219\n\t"
    "v_exp_f32 v18, v18\n\t"
    "v_exp_f32 v19, v19\n\t"
    "s_waitcnt lgkmcnt(0)\n\t"
    "v_exp_f32 v20, v20\n\t"
    "v_mfma_f32_32x32x16_bf16 v[0:15], v[136:139], v[128:131], v[96:111]\n\t"
    "v_exp_f32 v21, v21\n\t"
    "v_exp_f32 v22, v22\n\t"
    "v_exp_f32 v23, v23\n\t"
    "v_exp_f32 v24, v24\n\t"
    "v_exp_f32 v25, v25\n\t"
    "v_exp_f32 v26, v26\n\t"
    "v_smfmac_f32_32x32x32_bf16 v[172:187], v[140:143], v[16:23], v219\n\t"
    "v_exp_f32 v27, v27\n\t"
    "v_exp_f32 v28, v28\n\t"
    "v_exp_f32 v29, v29\n\t"
    "v_exp_f32 v30, v30\n\t"
    "v_exp_f32 v31, v31\n\t"
    "s_waitcnt vmcnt(6) lgkmcnt(0)\n\t"
    "s_barrier\n\t"
    "v_exp_f32 v0, v0\n\t"
    "v_exp_f32 v1, v1\n\t"
    "v_smfmac_f32_32x32x32_bf16 v[172:187], v[144:147], v[24:31], v219\n\t"
    "s_add_u32 m0, s57, 12288\n\t"
    "v_exp_f32 v2, v2\n\t"
    "buffer_load_dwordx4 v216, s[44:47], s53 offen sc1 lds\n\t"
    "v_exp_f32 v3, v3\n\t"
    "s_mov_b64 exec, s[60:61]\n\t"
    "ds_read_b128 v[140:143], v205 offset:36864\n\t"
    "ds_read_b128 v[144:147], v205 offset:37376\n\t"
    "s_mov_b64 exec, -1\n\t"
    "v_exp_f32 v4, v4\n\t"
    "v_mfma_f32_32x32x16_bf16 v[16:31], v[136:139], v[132:135], v[112:127]\n\t"
    "v_exp_f32 v5, v5\n\t"
    "v_exp_f32 v6, v6\n\t"
    "ds_read_b128 v[136:139], v204 offset:40960\n\t"
    "v_exp_f32 v7, v7\n\t"
    "v_exp_f32 v8, v8\n\t"
    "buffer_load_dwordx4 v217, s[44:47], s53 offen offset:2048 sc1 lds\n\t"
    "s_add_u32 s53, s53, 2048\n\t"
    "v_exp_f32 v9, v9\n\t"
    "v_exp_f32 v10, v10\n\t"
    "v_smfmac_f32_32x32x32_bf16 v[156:171], v[148:151], v[0:7], v219\n\t"
    "v_exp_f32 v11, v11\n\t"
    "s_add_u32 m0, s56, 16384\n\t"
    "v_exp_f32 v12, v12\n\t"
    "buffer_load_dwordx4 v212, s[40:43], s52 offen offset:0 nt lds\n\t"
    "v_exp_f32 v13, v13\n\t"
    "v_exp_f32 v14, v14\n\t"
    "v_exp_f32 v15, v15\n\t"
    "v_exp_f32 v16, v16\n\t"
    "buffer_load_dwordx4 v213, s[40:43], s52 offen offset:1024 nt lds\n\t"
    "v_exp_f32 v17, v17\n\t"
    "v_smfmac_f32_32x32x32_bf16 v[156:171], v[152:155], v[8:15], v219\n\t"
    "v_exp_f32 v18, v18\n\t"
    "v_exp_f32 v19, v19\n\t"
    "s_waitcnt lgkmcnt(0)\n\t"
    "v_exp_f32 v20, v20\n\t"
    "v_mfma_f32_32x32x16_bf16 v[0:15], v[136:139], v[128:131], v[96:111]\n\t"
    "v_exp_f32 v21, v21\n\t"
    "s_add_u32 m0, s58, 24576\n\t"
    "v_exp_f32 v22, v22\n\t"
    "buffer_load_dwordx4 v218, s[48:51], s54 offen sc1 lds\n\t"
    "s_add_u32 s54, s54, 2048\n\t"
    "v_exp_f32 v23, v23\n\t"
    "v_exp_f32 v24, v24\n\t"
    "v_exp_f32 v25, v25\n\t"
    "v_exp_f32 v26, v26\n\t"
    "v_smfmac_f32_32x32x32_bf16 v[172:187], v[148:151], v[16:23], v219\n\t"
    "v_exp_f32 v27, v27\n\t"
    "v_exp_f32 v28, v28\n\t"
    "v_exp_f32 v29, v29\n\t"
    "v_exp_f32 v30, v30\n\t"
    "v_exp_f32 v31, v31\n\t"
    "s_waitcnt vmcnt(8) lgkmcnt(0)\n\t"
    "s_barrier\n\t"
    "v_exp_f32 v0, v0\n\t"
    "v_exp_f32 v1, v1\n\t"
    "v_smfmac_f32_32x32x32_bf16 v[172:187], v[152:155], v[24:31], v219\n\t"
    "s_add_u32 m0, s57, 24576\n\t"
    "v_exp_f32 v2, v2\n\t"
    "buffer_load_dwordx4 v216, s[44:47], s53 offen sc1 lds\n\t"
    "v_exp_f32 v3, v3\n\t"
    "s_mov_b64 exec, s[60:61]\n\t"
    "ds_read_b128 v[148:151], v205 offset:0\n\t"
    "ds_read_b128 v[152:155], v205 offset:512\n\t"
    "s_mov_b64 exec, -1\n\t"
    "v_exp_f32 v4, v4\n\t"
    "v_mfma_f32_32x32x16_bf16 v[16:31], v[136:139], v[132:135], v[112:127]\n\t"
    "v_exp_f32 v5, v5\n\t"
    "v_exp_f32 v6, v6\n\t"
    "ds_read_b128 v[136:139], v204 offset:4096\n\t"
    "v_exp_f32 v7, v7\n\t"
    "v_exp_f32 v8, v8\n\t"
    "buffer_load_dwordx4 v217, s[44:47], s53 offen offset:2048 sc1 lds\n\t"
    "s_add_u32 s53, s53, 2048\n\t"
    "v_exp_f32 v9, v9\n\t"
    "v_exp_f32 v10, v10\n\t"
    "v_smfmac_f32_32x32x32_bf16 v[156:171], v[140:143], v[0:7], v219\n\t"
    "v_exp_f32 v11, v11\n\t"
    "s_add_u32 m0, s56, 16384\n\t"
    "v_exp_f32 v12, v12\n\t"
    "buffer_load_dwordx4 v214, s[40:43], s52 offen offset:2048 nt lds\n\t"
    "v_exp_f32 v13, v13\n\t"
    "v_exp_f32 v14, v14\n\t"
    "v_exp_f32 v15, v15\n\t"
    "v_exp_f32 v16, v16\n\t"
    "buffer_load_dwordx4 v215, s[40:43], s52 offen offset:3072 nt lds\n\t"
    "s_add_u32 s52, s52, 128\n\t"
    "v_exp_f32 v17, v17\n\t"
    "v_smfmac_f32_32x32x32_bf16 v[156:171], v[144:147], v[8:15], v219\n\t"
    "v_exp_f32 v18, v18\n\t"
    "v_exp_f32 v19, v19\n\t"
    "s_waitcnt lgkmcnt(0)\n\t"
    "v_exp_f32 v20, v20\n\t"
    "v_mfma_f32_32x32x16_bf16 v[0:15], v[136:139], v[128:131], v[96:111]\n\t"
    "v_exp_f32 v21, v21\n\t"
    "s_add_u32 m0, s58, 36864\n\t"
    "v_exp_f32 v22, v22\n\t"
    "buffer_load_dwordx4 v218, s[48:51], s54 offen sc1 lds\n\t"
    "s_add_u32 s54, s54, 2048\n\t"
    "v_exp_f32 v23, v23\n\t"
    "v_exp_f32 v24, v24\n\t"
    "v_exp_f32 v25, v25\n\t"
    "v_exp_f32 v26, v26\n\t"
    "v_smfmac_f32_32x32x32_bf16 v[172:187], v[140:143], v[16:23], v219\n\t"
    "v_exp_f32 v27, v27\n\t"
    "v_exp_f32 v28, v28\n\t"
    "v_exp_f32 v29, v29\n\t"
    "v_exp_f32 v30, v30\n\t"
    "v_exp_f32 v31, v31\n\t"
    "s_waitcnt vmcnt(8) lgkmcnt(0)\n\t"
    "s_barrier\n\t"
    "v_exp_f32 v0, v0\n\t"
    "v_exp_f32 v1, v1\n\t"
    "v_smfmac_f32_32x32x32_bf16 v[172:187], v[144:147], v[24:31], v219\n\t"
    "s_add_u32 m0, s57, 36864\n\t"
    "v_exp_f32 v2, v2\n\t"
    "buffer_load_dwordx4 v216, s[44:47], s53 offen sc1 lds\n\t"
    "v_exp_f32 v3, v3\n\t"
    "s_mov_b64 exec, s[60:61]\n\t"
    "ds_read_b128 v[140:143], v205 offset:12288\n\t"
    "ds_read_b128 v[144:147], v205 offset:12800\n\t"
    "s_mov_b64 exec, -1\n\t"
    "v_exp_f32 v4, v4\n\t"
    "v_mfma_f32_32x32x16_bf16 v[16:31], v[136:139], v[132:135], v[112:127]\n\t"
    "v_exp_f32 v5, v5\n\t"
    "v_exp_f32 v6, v6\n\t"
    "ds_read_b128 v[136:139], v204 offset:16384\n\t"
    "v_exp_f32 v7, v7\n\t"
    "v_exp_f32 v8, v8\n\t"
    "ds_read_b128 v[56:59], v206 offset:0\n\t"
    "buffer_load_dwordx4 v217, s[44:47], s53 offen offset:2048 sc1 lds\n\t"
    "s_add_u32 s53, s53, 2048\n\t"
    "v_exp_f32 v9, v9\n\t"
    "ds_read_b128 v[48:51], v204 offset:12288\n\t"
    "v_exp_f32 v10, v10\n\t"
    "v_smfmac_f32_32x32x32_bf16 v[156:171], v[148:151], v[0:7], v219\n\t"
    "v_exp_f32 v11, v11\n\t"
    "ds_read_b128 v[52:55], v204 offset:13312\n\t"
    "s_add_u32 m0, s56, 32768\n\t"
    "v_exp_f32 v12, v12\n\t"
    "ds_read_b128 v[68:71], v207 offset:0\n\t"
    "buffer_load_dwordx4 v212, s[40:43], s52 offen offset:0 nt lds\n\t"
    "v_exp_f32 v13, v13\n\t"
    "v_exp_f32 v14, v14\n\t"
    "ds_read_b128 v[60:63], v204 offset:14336\n\t"
    "v_exp_f32 v15, v15\n\t"
    "ds_read_b128 v[64:67], v204 offset:15360\n\t"
    "v_exp_f32 v16, v16\n\t"
    "buffer_load_dwordx4 v213, s[40:43], s52 offen offset:1024 nt lds\n\t"
    "v_exp_f32 v17, v17\n\t"
    "v_smfmac_f32_32x32x32_bf16 v[156:171], v[152:155], v[8:15], v219\n\t"
    "v_exp_f32 v18, v18\n\t"
    "v_exp_f32 v19, v19\n\t"
    "s_waitcnt lgkmcnt(6)\n\t"
    "v_exp_f32 v20, v20\n\t"
    "v_mfma_f32_32x32x16_bf16 v[0:15], v[136:139], v[128:131], v[96:111]\n\t"
    "v_exp_f32 v21, v21\n\t"
    "s_add_u32 m0, s58, 0\n\t"
    "v_exp_f32 v22, v22\n\t"
    "buffer_load_dwordx4 v218, s[48:51], s54 offen sc1 lds\n\t"
    "s_add_u32 s54, s54, 2048\n\t"
    "v_exp_f32 v23, v23\n\t"
    "v_exp_f32 v24, v24\n\t"
    "v_exp_f32 v25, v25\n\t"
    "v_exp_f32 v26, v26\n\t"
    "v_smfmac_f32_32x32x32_bf16 v[172:187], v[148:151], v[16:23], v219\n\t"
    "v_exp_f32 v27, v27\n\t"
    "v_exp_f32 v28, v28\n\t"
    "v_exp_f32 v29, v29\n\t"
    "v_exp_f32 v30, v30\n\t"
    "v_exp_f32 v31, v31\n\t"
    "s_waitcnt vmcnt(8) lgkmcnt(0)\n\t"
    "s_movk_i32 s59, 2\n\t"
    ".p2align 6\n\t"
    "2:\n\t"
    "s_barrier\n\t"
    "v_exp_f32 v0, v0\n\t"
    "v_exp_f32 v1, v1\n\t"
    "v_smfmac_f32_32x32x32_bf16 v[172:187], v[152:155], v[24:31], v219\n\t"
    "s_add_u32 m0, s57, 0\n\t"
    "v_exp_f32 v2, v2\n\t"
    "buffer_load_dwordx4 v216, s[44:47], s53 offen sc1 lds\n\t"
    "v_exp_f32 v3, v3\n\t"
    "s_mov_b64 exec, s[60:61]\n\t"
    "ds_read_b128 v[148:151], v205 offset:24576\n\t"
    "ds_read_b128 v[152:155], v205 offset:25088\n\t"
    "s_mov_b64 exec, -1\n\t"
    "v_exp_f32 v4, v4\n\t"
    "v_mfma_f32_32x32x16_bf16 v[16:31], v[136:139], v[132:135], v[112:127]\n\t"
    "v_exp_f32 v5, v5\n\t"
    "v_exp_f32 v6, v6\n\t"
    "ds_read_b128 v[136:139], v204 offset:28672\n\t"
    "v_exp_f32 v7, v7\n\t"
    "v_mfma_f32_32x32x16_bf16 v[188:203], v[48:51], v[56:59], v[188:203]\n\t"
    "v_exp_f32 v8, v8\n\t"
    "ds_read_b128 v[80:83], v208 offset:0\n\t"
    "buffer_load_dwordx4 v217, s[44:47], s53 offen offset:2048 sc1 lds\n\t"
    "s_add_u32 s53, s53, 2048\n\t"
    "v_exp_f32 v9, v9\n\t"
    "ds_read_b128 v[72:75], v204 offset:24576\n\t"
    "v_exp_f32 v10, v10\n\t"
    "v_smfmac_f32_32x32x32_bf16 v[156:171], v[140:143], v[0:7], v219\n\t"
    "v_exp_f32 v11, v11\n\t"
    "ds_read_b128 v[76:79], v204 offset:25600\n\t"
    "s_add_u32 m0, s56, 32768\n\t"
    "v_exp_f32 v12, v12\n\t"
    "ds_read_b128 v[92:95], v209 offset:0\n\t"
    "buffer_load_dwordx4 v214, s[40:43], s52 offen offset:2048 nt lds\n\t"
    "v_exp_f32 v13, v13\n\t"
    "v_mfma_f32_32x32x16_bf16 v[238:253], v[52:55], v[56:59], v[238:253]\n\t"
    "v_exp_f32 v14, v14\n\t"
    "ds_read_b128 v[84:87], v204 offset:26624\n\t"
    "v_exp_f32 v15, v15\n\t"
    "ds_read_b128 v[88:91], v204 offset:27648\n\t"
    "v_exp_f32 v16, v16\n\t"
    "buffer_load_dwordx4 v215, s[40:43], s52 offen offset:3072 nt lds\n\t"
    "s_add_u32 s52, s52, 128\n\t"
    "v_exp_f32 v17, v17\n\t"
    "v_smfmac_f32_32x32x32_bf16 v[156:171], v[144:147], v[8:15], v219\n\t"
    "v_exp_f32 v18, v18\n\t"
    "v_exp_f32 v19, v19\n\t"
    "s_waitcnt lgkmcnt(6)\n\t"
    "v_exp_f32 v20, v20\n\t"
    "v_mfma_f32_32x32x16_bf16 v[0:15], v[136:139], v[128:131], v[96:111]\n\t"
    "v_exp_f32 v21, v21\n\t"
    "s_add_u32 m0, s58, 12288\n\t"
    "v_exp_f32 v22, v22\n\t"
    "buffer_load_dwordx4 v218, s[48:51], s54 offen sc1 lds\n\t"
    "s_add_u32 s54, s54, 2048\n\t"
    "v_exp_f32 v23, v23\n\t"
    "v_mfma_f32_32x32x16_bf16 v[188:203], v[60:63], v[68:71], v[188:203]\n\t"
    "v_exp_f32 v24, v24\n\t"
    "v_exp_f32 v25, v25\n\t"
    "v_exp_f32 v26, v26\n\t"
    "v_smfmac_f32_32x32x32_bf16 v[172:187], v[140:143], v[16:23], v219\n\t"
    "v_exp_f32 v27, v27\n\t"
    "v_exp_f32 v28, v28\n\t"
    "v_exp_f32 v29, v29\n\t"
    "v_mfma_f32_32x32x16_bf16 v[238:253], v[64:67], v[68:71], v[238:253]\n\t"
    "v_exp_f32 v30, v30\n\t"
    "v_exp_f32 v31, v31\n\t"
    "s_waitcnt vmcnt(8) lgkmcnt(0)\n\t"
    "s_barrier\n\t"
    "v_exp_f32 v0, v0\n\t"
    "v_exp_f32 v1, v1\n\t"
    "v_smfmac_f32_32x32x32_bf16 v[172:187], v[144:147], v[24:31], v219\n\t"
    "s_add_u32 m0, s57, 12288\n\t"
    "v_exp_f32 v2, v2\n\t"
    "buffer_load_dwordx4 v216, s[44:47], s53 offen sc1 lds\n\t"
    "v_exp_f32 v3, v3\n\t"
    "s_mov_b64 exec, s[60:61]\n\t"
    "ds_read_b128 v[140:143], v205 offset:36864\n\t"
    "ds_read_b128 v[144:147], v205 offset:37376\n\t"
    "s_mov_b64 exec, -1\n\t"
    "v_exp_f32 v4, v4\n\t"
    "v_mfma_f32_32x32x16_bf16 v[16:31], v[136:139], v[132:135], v[112:127]\n\t"
    "v_exp_f32 v5, v5\n\t"
    "v_exp_f32 v6, v6\n\t"
    "ds_read_b128 v[136:139], v204 offset:40960\n\t"
    "v_exp_f32 v7, v7\n\t"
    "v_mfma_f32_32x32x16_bf16 v[188:203], v[72:75], v[80:83], v[188:203]\n\t"
    "v_exp_f32 v8, v8\n\t"
    "ds_read_b128 v[56:59], v206 offset:16384\n\t"
    "buffer_load_dwordx4 v217, s[44:47], s53 offen offset:2048 sc1 lds\n\t"
    "s_add_u32 s53, s53, 2048\n\t"
    "v_exp_f32 v9, v9\n\t"
    "ds_read_b128 v[48:51], v204 offset:36864\n\t"
    "v_exp_f32 v10, v10\n\t"
    "v_smfmac_f32_32x32x32_bf16 v[156:171], v[148:151], v[0:7], v219\n\t"
    "v_exp_f32 v11, v11\n\t"
    "ds_read_b128 v[52:55], v204 offset:37888\n\t"
    "s_add_u32 m0, s56, 0\n\t"
    "v_exp_f32 v12, v12\n\t"
    "ds_read_b128 v[68:71], v207 offset:16384\n\t"
    "buffer_load_dwordx4 v212, s[40:43], s52 offen offset:0 nt lds\n\t"
    "v_exp_f32 v13, v13\n\t"
    "v_mfma_f32_32x32x16_bf16 v[238:253], v[76:79], v[80:83], v[238:253]\n\t"
    "v_exp_f32 v14, v14\n\t"
    "ds_read_b128 v[60:63], v204 offset:38912\n\t"
    "v_exp_f32 v15, v15\n\t"
    "ds_read_b128 v[64:67], v204 offset:39936\n\t"
    "v_exp_f32 v16, v16\n\t"
    "buffer_load_dwordx4 v213, s[40:43], s52 offen offset:1024 nt lds\n\t"
    "v_exp_f32 v17, v17\n\t"
    "v_smfmac_f32_32x32x32_bf16 v[156:171], v[152:155], v[8:15], v219\n\t"
    "v_exp_f32 v18, v18\n\t"
    "v_exp_f32 v19, v19\n\t"
    "s_waitcnt lgkmcnt(6)\n\t"
    "v_exp_f32 v20, v20\n\t"
    "v_mfma_f32_32x32x16_bf16 v[0:15], v[136:139], v[128:131], v[96:111]\n\t"
    "v_exp_f32 v21, v21\n\t"
    "s_add_u32 m0, s58, 24576\n\t"
    "v_exp_f32 v22, v22\n\t"
    "buffer_load_dwordx4 v218, s[48:51], s54 offen sc1 lds\n\t"
    "s_add_u32 s54, s54, 2048\n\t"
    "v_exp_f32 v23, v23\n\t"
    "v_mfma_f32_32x32x16_bf16 v[188:203], v[84:87], v[92:95], v[188:203]\n\t"
    "v_exp_f32 v24, v24\n\t"
    "v_exp_f32 v25, v25\n\t"
    "v_exp_f32 v26, v26\n\t"
    "v_smfmac_f32_32x32x32_bf16 v[172:187], v[148:151], v[16:23], v219\n\t"
    "v_exp_f32 v27, v27\n\t"
    "v_exp_f32 v28, v28\n\t"
    "v_exp_f32 v29, v29\n\t"
    "v_mfma_f32_32x32x16_bf16 v[238:253], v[88:91], v[92:95], v[238:253]\n\t"
    "v_exp_f32 v30, v30\n\t"
    "v_exp_f32 v31, v31\n\t"
    "s_waitcnt vmcnt(8) lgkmcnt(0)\n\t"
    "s_barrier\n\t"
    "v_exp_f32 v0, v0\n\t"
    "v_exp_f32 v1, v1\n\t"
    "v_smfmac_f32_32x32x32_bf16 v[172:187], v[152:155], v[24:31], v219\n\t"
    "s_add_u32 m0, s57, 24576\n\t"
    "v_exp_f32 v2, v2\n\t"
    "buffer_load_dwordx4 v216, s[44:47], s53 offen sc1 lds\n\t"
    "v_exp_f32 v3, v3\n\t"
    "s_mov_b64 exec, s[60:61]\n\t"
    "ds_read_b128 v[148:151], v205 offset:0\n\t"
    "ds_read_b128 v[152:155], v205 offset:512\n\t"
    "s_mov_b64 exec, -1\n\t"
    "v_exp_f32 v4, v4\n\t"
    "v_mfma_f32_32x32x16_bf16 v[16:31], v[136:139], v[132:135], v[112:127]\n\t"
    "v_exp_f32 v5, v5\n\t"
    "v_exp_f32 v6, v6\n\t"
    "ds_read_b128 v[136:139], v204 offset:4096\n\t"
    "v_exp_f32 v7, v7\n\t"
    "v_mfma_f32_32x32x16_bf16 v[188:203], v[48:51], v[56:59], v[188:203]\n\t"
    "v_exp_f32 v8, v8\n\t"
    "ds_read_b128 v[80:83], v208 offset:16384\n\t"
    "buffer_load_dwordx4 v217, s[44:47], s53 offen offset:2048 sc1 lds\n\t"
    "s_add_u32 s53, s53, 2048\n\t"
    "v_exp_f32 v9, v9\n\t"
    "ds_read_b128 v[72:75], v204 offset:0\n\t"
    "v_exp_f32 v10, v10\n\t"
    "v_smfmac_f32_32x32x32_bf16 v[156:171], v[140:143], v[0:7], v219\n\t"
    "v_exp_f32 v11, v11\n\t"
    "ds_read_b128 v[76:79], v204 offset:1024\n\t"
    "s_add_u32 m0, s56, 0\n\t"
    "v_exp_f32 v12, v12\n\t"
    "ds_read_b128 v[92:95], v209 offset:16384\n\t"
    "buffer_load_dwordx4 v214, s[40:43], s52 offen offset:2048 nt lds\n\t"
    "v_exp_f32 v13, v13\n\t"
    "v_mfma_f32_32x32x16_bf16 v[238:253], v[52:55], v[56:59], v[238:253]\n\t"
    "v_exp_f32 v14, v14\n\t"
    "ds_read_b128 v[84:87], v204 offset:2048\n\t"
    "v_exp_f32 v15, v15\n\t"
    "ds_read_b128 v[88:91], v204 offset:3072\n\t"
    "v_exp_f32 v16, v16\n\t"
    "buffer_load_dwordx4 v215, s[40:43], s52 offen offset:3072 nt lds\n\t"
    "s_add_u32 s52, s52, 128\n\t"
    "v_exp_f32 v17, v17\n\t"
    "v_smfmac_f32_32x32x32_bf16 v[156:171], v[144:147], v[8:15], v219\n\t"
    "v_exp_f32 v18, v18\n\t"
    "v_exp_f32 v19, v19\n\t"
    "s_waitcnt lgkmcnt(6)\n\t"
    "v_exp_f32 v20, v20\n\t"
    "v_mfma_f32_32x32x16_bf16 v[0:15], v[136:139], v[128:131], v[96:111]\n\t"
    "v_exp_f32 v21, v21\n\t"
    "s_add_u32 m0, s58, 36864\n\t"
    "v_exp_f32 v22, v22\n\t"
    "buffer_load_dwordx4 v218, s[48:51], s54 offen sc1 lds\n\t"
    "s_add_u32 s54, s54, 2048\n\t"
    "v_exp_f32 v23, v23\n\t"
    "v_mfma_f32_32x32x16_bf16 v[188:203], v[60:63], v[68:71], v[188:203]\n\t"
    "v_exp_f32 v24, v24\n\t"
    "v_exp_f32 v25, v25\n\t"
    "v_exp_f32 v26, v26\n\t"
    "v_smfmac_f32_32x32x32_bf16 v[172:187], v[140:143], v[16:23], v219\n\t"
    "v_exp_f32 v27, v27\n\t"
    "v_exp_f32 v28, v28\n\t"
    "v_exp_f32 v29, v29\n\t"
    "v_mfma_f32_32x32x16_bf16 v[238:253], v[64:67], v[68:71], v[238:253]\n\t"
    "v_exp_f32 v30, v30\n\t"
    "v_exp_f32 v31, v31\n\t"
    "s_waitcnt vmcnt(8) lgkmcnt(0)\n\t"
    "s_barrier\n\t"
    "v_exp_f32 v0, v0\n\t"
    "v_exp_f32 v1, v1\n\t"
    "v_smfmac_f32_32x32x32_bf16 v[172:187], v[144:147], v[24:31], v219\n\t"
    "s_add_u32 m0, s57, 36864\n\t"
    "v_exp_f32 v2, v2\n\t"
    "buffer_load_dwordx4 v216, s[44:47], s53 offen sc1 lds\n\t"
    "v_exp_f32 v3, v3\n\t"
    "s_mov_b64 exec, s[60:61]\n\t"
    "ds_read_b128 v[140:143], v205 offset:12288\n\t"
    "ds_read_b128 v[144:147], v205 offset:12800\n\t"
    "s_mov_b64 exec, -1\n\t"
    "v_exp_f32 v4, v4\n\t"
    "v_mfma_f32_32x32x16_bf16 v[16:31], v[136:139], v[132:135], v[112:127]\n\t"
    "v_exp_f32 v5, v5\n\t"
    "v_exp_f32 v6, v6\n\t"
    "ds_read_b128 v[136:139], v204 offset:16384\n\t"
    "v_exp_f32 v7, v7\n\t"
    "v_mfma_f32_32x32x16_bf16 v[188:203], v[72:75], v[80:83], v[188:203]\n\t"
    "v_exp_f32 v8, v8\n\t"
    "ds_read_b128 v[56:59], v206 offset:32768\n\t"
    "buffer_load_dwordx4 v217, s[44:47], s53 offen offset:2048 sc1 lds\n\t"
    "s_add_u32 s53, s53, 2048\n\t"
    "v_exp_f32 v9, v9\n\t"
    "ds_read_b128 v[48:51], v204 offset:12288\n\t"
    "v_exp_f32 v10, v10\n\t"
    "v_smfmac_f32_32x32x32_bf16 v[156:171], v[148:151], v[0:7], v219\n\t"
    "v_exp_f32 v11, v11\n\t"
    "ds_read_b128 v[52:55], v204 offset:13312\n\t"
    "s_add_u32 m0, s56, 16384\n\t"
    "v_exp_f32 v12, v12\n\t"
    "ds_read_b128 v[68:71], v207 offset:32768\n\t"
    "buffer_load_dwordx4 v212, s[40:43], s52 offen offset:0 nt lds\n\t"
    "v_exp_f32 v13, v13\n\t"
    "v_mfma_f32_32x32x16_bf16 v[238:253], v[76:79], v[80:83], v[238:253]\n\t"
    "v_exp_f32 v14, v14\n\t"
    "ds_read_b128 v[60:63], v204 offset:14336\n\t"
    "v_exp_f32 v15, v15\n\t"
    "ds_read_b128 v[64:67], v204 offset:15360\n\t"
    "v_exp_f32 v16, v16\n\t"
    "buffer_load_dwordx4 v213, s[40:43], s52 offen offset:1024 nt lds\n\t"
    "v_exp_f32 v17, v17\n\t"
    "v_smfmac_f32_32x32x32_bf16 v[156:171], v[152:155], v[8:15], v219\n\t"
    "v_exp_f32 v18, v18\n\t"
    "v_exp_f32 v19, v19\n\t"
    "s_waitcnt lgkmcnt(6)\n\t"
    "v_exp_f32 v20, v20\n\t"
    "v_mfma_f32_32x32x16_bf16 v[0:15], v[136:139], v[128:131], v[96:111]\n\t"
    "v_exp_f32 v21, v21\n\t"
    "s_add_u32 m0, s58, 0\n\t"
    "v_exp_f32 v22, v22\n\t"
    "buffer_load_dwordx4 v218, s[48:51], s54 offen sc1 lds\n\t"
    "s_add_u32 s54, s54, 2048\n\t"
    "v_exp_f32 v23, v23\n\t"
    "v_mfma_f32_32x32x16_bf16 v[188:203], v[84:87], v[92:95], v[188:203]\n\t"
    "v_exp_f32 v24, v24\n\t"
    "v_exp_f32 v25, v25\n\t"
    "v_exp_f32 v26, v26\n\t"
    "v_smfmac_f32_32x32x32_bf16 v[172:187], v[148:151], v[16:23], v219\n\t"
    "v_exp_f32 v27, v27\n\t"
    "v_exp_f32 v28, v28\n\t"
    "v_exp_f32 v29, v29\n\t"
    "v_mfma_f32_32x32x16_bf16 v[238:253], v[88:91], v[92:95], v[238:253]\n\t"
    "v_exp_f32 v30, v30\n\t"
    "v_exp_f32 v31, v31\n\t"
    "s_waitcnt vmcnt(8) lgkmcnt(0)\n\t"
    "s_barrier\n\t"
    "v_exp_f32 v0, v0\n\t"
    "v_exp_f32 v1, v1\n\t"
    "v_smfmac_f32_32x32x32_bf16 v[172:187], v[152:155], v[24:31], v219\n\t"
    "s_add_u32 m0, s57, 0\n\t"
    "v_exp_f32 v2, v2\n\t"
    "buffer_load_dwordx4 v216, s[44:47], s53 offen sc1 lds\n\t"
    "v_exp_f32 v3, v3\n\t"
    "s_mov_b64 exec, s[60:61]\n\t"
    "ds_read_b128 v[148:151], v205 offset:24576\n\t"
    "ds_read_b128 v[152:155], v205 offset:25088\n\t"
    "s_mov_b64 exec, -1\n\t"
    "v_exp_f32 v4, v4\n\t"
    "v_mfma_f32_32x32x16_bf16 v[16:31], v[136:139], v[132:135], v[112:127]\n\t"
    "v_exp_f32 v5, v5\n\t"
    "v_exp_f32 v6, v6\n\t"
    "ds_read_b128 v[136:139], v204 offset:28672\n\t"
    "v_exp_f32 v7, v7\n\t"
    "v_mfma_f32_32x32x16_bf16 v[188:203], v[48:51], v[56:59], v[188:203]\n\t"
    "v_exp_f32 v8, v8\n\t"
    "ds_read_b128 v[80:83], v208 offset:32768\n\t"
    "buffer_load_dwordx4 v217, s[44:47], s53 offen offset:2048 sc1 lds\n\t"
    "s_add_u32 s53, s53, 2048\n\t"
    "v_exp_f32 v9, v9\n\t"
    "ds_read_b128 v[72:75], v204 offset:24576\n\t"
    "v_exp_f32 v10, v10\n\t"
    "v_smfmac_f32_32x32x32_bf16 v[156:171], v[140:143], v[0:7], v219\n\t"
    "v_exp_f32 v11, v11\n\t"
    "ds_read_b128 v[76:79], v204 offset:25600\n\t"
    "s_add_u32 m0, s56, 16384\n\t"
    "v_exp_f32 v12, v12\n\t"
    "ds_read_b128 v[92:95], v209 offset:32768\n\t"
    "buffer_load_dwordx4 v214, s[40:43], s52 offen offset:2048 nt lds\n\t"
    "v_exp_f32 v13, v13\n\t"
    "v_mfma_f32_32x32x16_bf16 v[238:253], v[52:55], v[56:59], v[238:253]\n\t"
    "v_exp_f32 v14, v14\n\t"
    "ds_read_b128 v[84:87], v204 offset:26624\n\t"
    "v_exp_f32 v15, v15\n\t"
    "ds_read_b128 v[88:91], v204 offset:27648\n\t"
    "v_exp_f32 v16, v16\n\t"
    "buffer_load_dwordx4 v215, s[40:43], s52 offen offset:3072 nt lds\n\t"
    "s_add_u32 s52, s52, 128\n\t"
    "v_exp_f32 v17, v17\n\t"
    "v_smfmac_f32_32x32x32_bf16 v[156:171], v[144:147], v[8:15], v219\n\t"
    "v_exp_f32 v18, v18\n\t"
    "v_exp_f32 v19, v19\n\t"
    "s_waitcnt lgkmcnt(6)\n\t"
    "v_exp_f32 v20, v20\n\t"
    "v_mfma_f32_32x32x16_bf16 v[0:15], v[136:139], v[128:131], v[96:111]\n\t"
    "v_exp_f32 v21, v21\n\t"
    "s_add_u32 m0, s58, 12288\n\t"
    "v_exp_f32 v22, v22\n\t"
    "buffer_load_dwordx4 v218, s[48:51], s54 offen sc1 lds\n\t"
    "s_add_u32 s54, s54, 2048\n\t"
    "v_exp_f32 v23, v23\n\t"
    "v_mfma_f32_32x32x16_bf16 v[188:203], v[60:63], v[68:71], v[188:203]\n\t"
    "v_exp_f32 v24, v24\n\t"
    "v_exp_f32 v25, v25\n\t"
    "v_exp_f32 v26, v26\n\t"
    "v_smfmac_f32_32x32x32_bf16 v[172:187], v[140:143], v[16:23], v219\n\t"
    "v_exp_f32 v27, v27\n\t"
    "v_exp_f32 v28, v28\n\t"
    "v_exp_f32 v29, v29\n\t"
    "v_mfma_f32_32x32x16_bf16 v[238:253], v[64:67], v[68:71], v[238:253]\n\t"
    "v_exp_f32 v30, v30\n\t"
    "v_exp_f32 v31, v31\n\t"
    "s_waitcnt vmcnt(8) lgkmcnt(0)\n\t"
    "s_barrier\n\t"
    "v_exp_f32 v0, v0\n\t"
    "v_exp_f32 v1, v1\n\t"
    "v_smfmac_f32_32x32x32_bf16 v[172:187], v[144:147], v[24:31], v219\n\t"
    "s_add_u32 m0, s57, 12288\n\t"
    "v_exp_f32 v2, v2\n\t"
    "buffer_load_dwordx4 v216, s[44:47], s53 offen sc1 lds\n\t"
    "v_exp_f32 v3, v3\n\t"
    "s_mov_b64 exec, s[60:61]\n\t"
    "ds_read_b128 v[140:143], v205 offset:36864\n\t"
    "ds_read_b128 v[144:147], v205 offset:37376\n\t"
    "s_mov_b64 exec, -1\n\t"
    "v_exp_f32 v4, v4\n\t"
    "v_mfma_f32_32x32x16_bf16 v[16:31], v[136:139], v[132:135], v[112:127]\n\t"
    "v_exp_f32 v5, v5\n\t"
    "v_exp_f32 v6, v6\n\t"
    "ds_read_b128 v[136:139], v204 offset:40960\n\t"
    "v_exp_f32 v7, v7\n\t"
    "v_mfma_f32_32x32x16_bf16 v[188:203], v[72:75], v[80:83], v[188:203]\n\t"
    "v_exp_f32 v8, v8\n\t"
    "ds_read_b128 v[56:59], v206 offset:0\n\t"
    "buffer_load_dwordx4 v217, s[44:47], s53 offen offset:2048 sc1 lds\n\t"
    "s_add_u32 s53, s53, 2048\n\t"
    "v_exp_f32 v9, v9\n\t"
    "ds_read_b128 v[48:51], v204 offset:36864\n\t"
    "v_exp_f32 v10, v10\n\t"
    "v_smfmac_f32_32x32x32_bf16 v[156:171], v[148:151], v[0:7], v219\n\t"
    "v_exp_f32 v11, v11\n\t"
    "ds_read_b128 v[52:55], v204 offset:37888\n\t"
    "s_add_u32 m0, s56, 32768\n\t"
    "v_exp_f32 v12, v12\n\t"
    "ds_read_b128 v[68:71], v207 offset:0\n\t"
    "buffer_load_dwordx4 v212, s[40:43], s52 offen offset:0 nt lds\n\t"
    "v_exp_f32 v13, v13\n\t"
    "v_mfma_f32_32x32x16_bf16 v[238:253], v[76:79], v[80:83], v[238:253]\n\t"
    "v_exp_f32 v14, v14\n\t"
    "ds_read_b128 v[60:63], v204 offset:38912\n\t"
    "v_exp_f32 v15, v15\n\t"
    "ds_read_b128 v[64:67], v204 offset:39936\n\t"
    "v_exp_f32 v16, v16\n\t"
    "buffer_load_dwordx4 v213, s[40:43], s52 offen offset:1024 nt lds\n\t"
    "v_exp_f32 v17, v17\n\t"
    "v_smfmac_f32_32x32x32_bf16 v[156:171], v[152:155], v[8:15], v219\n\t"
    "v_exp_f32 v18, v18\n\t"
    "v_exp_f32 v19, v19\n\t"
    "s_waitcnt lgkmcnt(6)\n\t"
    "v_exp_f32 v20, v20\n\t"
    "v_mfma_f32_32x32x16_bf16 v[0:15], v[136:139], v[128:131], v[96:111]\n\t"
    "v_exp_f32 v21, v21\n\t"
    "s_add_u32 m0, s58, 24576\n\t"
    "v_exp_f32 v22, v22\n\t"
    "buffer_load_dwordx4 v218, s[48:51], s54 offen sc1 lds\n\t"
    "s_add_u32 s54, s54, 2048\n\t"
    "v_exp_f32 v23, v23\n\t"
    "v_mfma_f32_32x32x16_bf16 v[188:203], v[84:87], v[92:95], v[188:203]\n\t"
    "v_exp_f32 v24, v24\n\t"
    "v_exp_f32 v25, v25\n\t"
    "v_exp_f32 v26, v26\n\t"
    "v_smfmac_f32_32x32x32_bf16 v[172:187], v[148:151], v[16:23], v219\n\t"
    "v_exp_f32 v27, v27\n\t"
    "v_exp_f32 v28, v28\n\t"
    "v_exp_f32 v29, v29\n\t"
    "v_mfma_f32_32x32x16_bf16 v[238:253], v[88:91], v[92:95], v[238:253]\n\t"
    "v_exp_f32 v30, v30\n\t"
    "v_exp_f32 v31, v31\n\t"
    "s_waitcnt vmcnt(8) lgkmcnt(0)\n\t"
    "s_barrier\n\t"
    "v_exp_f32 v0, v0\n\t"
    "v_exp_f32 v1, v1\n\t"
    "v_smfmac_f32_32x32x32_bf16 v[172:187], v[152:155], v[24:31], v219\n\t"
    "s_add_u32 m0, s57, 24576\n\t"
    "v_exp_f32 v2, v2\n\t"
    "buffer_load_dwordx4 v216, s[44:47], s53 offen sc1 lds\n\t"
    "v_exp_f32 v3, v3\n\t"
    "s_mov_b64 exec, s[60:61]\n\t"
    "ds_read_b128 v[148:151], v205 offset:0\n\t"
    "ds_read_b128 v[152:155], v205 offset:512\n\t"
    "s_mov_b64 exec, -1\n\t"
    "v_exp_f32 v4, v4\n\t"
    "v_mfma_f32_32x32x16_bf16 v[16:31], v[136:139], v[132:135], v[112:127]\n\t"
    "v_exp_f32 v5, v5\n\t"
    "v_exp_f32 v6, v6\n\t"
    "ds_read_b128 v[136:139], v204 offset:4096\n\t"
    "v_exp_f32 v7, v7\n\t"
    "v_mfma_f32_32x32x16_bf16 v[188:203], v[48:51], v[56:59], v[188:203]\n\t"
    "v_exp_f32 v8, v8\n\t"
    "ds_read_b128 v[80:83], v208 offset:0\n\t"
    "buffer_load_dwordx4 v217, s[44:47], s53 offen offset:2048 sc1 lds\n\t"
    "s_add_u32 s53, s53, 2048\n\t"
    "v_exp_f32 v9, v9\n\t"
    "ds_read_b128 v[72:75], v204 offset:0\n\t"
    "v_exp_f32 v10, v10\n\t"
    "v_smfmac_f32_32x32x32_bf16 v[156:171], v[140:143], v[0:7], v219\n\t"
    "v_exp_f32 v11, v11\n\t"
    "ds_read_b128 v[76:79], v204 offset:1024\n\t"
    "s_add_u32 m0, s56, 32768\n\t"
    "v_exp_f32 v12, v12\n\t"
    "ds_read_b128 v[92:95], v209 offset:0\n\t"
    "buffer_load_dwordx4 v214, s[40:43], s52 offen offset:2048 nt lds\n\t"
    "v_exp_f32 v13, v13\n\t"
    "v_mfma_f32_32x32x16_bf16 v[238:253], v[52:55], v[56:59], v[238:253]\n\t"
    "v_exp_f32 v14, v14\n\t"
    "ds_read_b128 v[84:87], v204 offset:2048\n\t"
    "v_exp_f32 v15, v15\n\t"
    "ds_read_b128 v[88:91], v204 offset:3072\n\t"
    "v_exp_f32 v16, v16\n\t"
    "buffer_load_dwordx4 v215, s[40:43], s52 offen offset:3072 nt lds\n\t"
    "s_add_u32 s52, s52, 128\n\t"
    "v_exp_f32 v17, v17\n\t"
    "v_smfmac_f32_32x32x32_bf16 v[156:171], v[144:147], v[8:15], v219\n\t"
    "v_exp_f32 v18, v18\n\t"
    "v_exp_f32 v19, v19\n\t"
    "s_waitcnt lgkmcnt(6)\n\t"
    "v_exp_f32 v20, v20\n\t"
    "v_mfma_f32_32x32x16_bf16 v[0:15], v[136:139], v[128:131], v[96:111]\n\t"
    "v_exp_f32 v21, v21\n\t"
    "s_add_u32 m0, s58, 36864\n\t"
    "v_exp_f32 v22, v22\n\t"
    "buffer_load_dwordx4 v218, s[48:51], s54 offen sc1 lds\n\t"
    "s_add_u32 s54, s54, 2048\n\t"
    "v_exp_f32 v23, v23\n\t"
    "v_mfma_f32_32x32x16_bf16 v[188:203], v[60:63], v[68:71], v[188:203]\n\t"
    "v_exp_f32 v24, v24\n\t"
    "v_exp_f32 v25, v25\n\t"
    "v_exp_f32 v26, v26\n\t"
    "v_smfmac_f32_32x32x32_bf16 v[172:187], v[140:143], v[16:23], v219\n\t"
    "v_exp_f32 v27, v27\n\t"
    "v_exp_f32 v28, v28\n\t"
    "v_exp_f32 v29, v29\n\t"
    "v_mfma_f32_32x32x16_bf16 v[238:253], v[64:67], v[68:71], v[238:253]\n\t"
    "v_exp_f32 v30, v30\n\t"
    "v_exp_f32 v31, v31\n\t"
    "s_waitcnt vmcnt(8) lgkmcnt(0)\n\t"
    "s_barrier\n\t"
    "v_exp_f32 v0, v0\n\t"
    "v_exp_f32 v1, v1\n\t"
    "v_smfmac_f32_32x32x32_bf16 v[172:187], v[144:147], v[24:31], v219\n\t"
    "s_add_u32 m0, s57, 36864\n\t"
    "v_exp_f32 v2, v2\n\t"
    "buffer_load_dwordx4 v216, s[44:47], s53 offen sc1 lds\n\t"
    "v_exp_f32 v3, v3\n\t"
    "s_mov_b64 exec, s[60:61]\n\t"
    "ds_read_b128 v[140:143], v205 offset:12288\n\t"
    "ds_read_b128 v[144:147], v205 offset:12800\n\t"
    "s_mov_b64 exec, -1\n\t"
    "v_exp_f32 v4, v4\n\t"
    "v_mfma_f32_32x32x16_bf16 v[16:31], v[136:139], v[132:135], v[112:127]\n\t"
    "v_exp_f32 v5, v5\n\t"
    "v_exp_f32 v6, v6\n\t"
    "ds_read_b128 v[136:139], v204 offset:16384\n\t"
    "v_exp_f32 v7, v7\n\t"
    "v_mfma_f32_32x32x16_bf16 v[188:203], v[72:75], v[80:83], v[188:203]\n\t"
    "v_exp_f32 v8, v8\n\t"
    "ds_read_b128 v[56:59], v206 offset:16384\n\t"
    "buffer_load_dwordx4 v217, s[44:47], s53 offen offset:2048 sc1 lds\n\t"
    "s_add_u32 s53, s53, 2048\n\t"
    "v_exp_f32 v9, v9\n\t"
    "ds_read_b128 v[48:51], v204 offset:12288\n\t"
    "v_exp_f32 v10, v10\n\t"
    "v_smfmac_f32_32x32x32_bf16 v[156:171], v[148:151], v[0:7], v219\n\t"
    "v_exp_f32 v11, v11\n\t"
    "ds_read_b128 v[52:55], v204 offset:13312\n\t"
    "s_add_u32 m0, s56, 0\n\t"
    "v_exp_f32 v12, v12\n\t"
    "ds_read_b128 v[68:71], v207 offset:16384\n\t"
    "buffer_load_dwordx4 v212, s[40:43], s52 offen offset:0 nt lds\n\t"
    "v_exp_f32 v13, v13\n\t"
    "v_mfma_f32_32x32x16_bf16 v[238:253], v[76:79], v[80:83], v[238:253]\n\t"
    "v_exp_f32 v14, v14\n\t"
    "ds_read_b128 v[60:63], v204 offset:14336\n\t"
    "v_exp_f32 v15, v15\n\t"
    "ds_read_b128 v[64:67], v204 offset:15360\n\t"
    "v_exp_f32 v16, v16\n\t"
    "buffer_load_dwordx4 v213, s[40:43], s52 offen offset:1024 nt lds\n\t"
    "v_exp_f32 v17, v17\n\t"
    "v_smfmac_f32_32x32x32_bf16 v[156:171], v[152:155], v[8:15], v219\n\t"
    "v_exp_f32 v18, v18\n\t"
    "v_exp_f32 v19, v19\n\t"
    "s_waitcnt lgkmcnt(6)\n\t"
    "v_exp_f32 v20, v20\n\t"
    "v_mfma_f32_32x32x16_bf16 v[0:15], v[136:139], v[128:131], v[96:111]\n\t"
    "v_exp_f32 v21, v21\n\t"
    "s_add_u32 m0, s58, 0\n\t"
    "v_exp_f32 v22, v22\n\t"
    "buffer_load_dwordx4 v218, s[48:51], s54 offen sc1 lds\n\t"
    "s_add_u32 s54, s54, 2048\n\t"
    "v_exp_f32 v23, v23\n\t"
    "v_mfma_f32_32x32x16_bf16 v[188:203], v[84:87], v[92:95], v[188:203]\n\t"
    "v_exp_f32 v24, v24\n\t"
    "v_exp_f32 v25, v25\n\t"
    "v_exp_f32 v26, v26\n\t"
    "v_smfmac_f32_32x32x32_bf16 v[172:187], v[148:151], v[16:23], v219\n\t"
    "v_exp_f32 v27, v27\n\t"
    "v_exp_f32 v28, v28\n\t"
    "v_exp_f32 v29, v29\n\t"
    "v_mfma_f32_32x32x16_bf16 v[238:253], v[88:91], v[92:95], v[238:253]\n\t"
    "v_exp_f32 v30, v30\n\t"
    "v_exp_f32 v31, v31\n\t"
    "s_waitcnt vmcnt(8) lgkmcnt(0)\n\t"
    "s_barrier\n\t"
    "v_exp_f32 v0, v0\n\t"
    "s_cmp_eq_u32 s59, 1\n\t"
    "s_cselect_b32 s50, 0, s50\n\t"
    "v_exp_f32 v1, v1\n\t"
    "v_smfmac_f32_32x32x32_bf16 v[172:187], v[152:155], v[24:31], v219\n\t"
    "s_add_u32 m0, s57, 0\n\t"
    "v_exp_f32 v2, v2\n\t"
    "buffer_load_dwordx4 v216, s[44:47], s53 offen sc1 lds\n\t"
    "v_exp_f32 v3, v3\n\t"
    "s_mov_b64 exec, s[60:61]\n\t"
    "ds_read_b128 v[148:151], v205 offset:24576\n\t"
    "ds_read_b128 v[152:155], v205 offset:25088\n\t"
    "s_mov_b64 exec, -1\n\t"
    "v_exp_f32 v4, v4\n\t"
    "v_mfma_f32_32x32x16_bf16 v[16:31], v[136:139], v[132:135], v[112:127]\n\t"
    "v_exp_f32 v5, v5\n\t"
    "v_exp_f32 v6, v6\n\t"
    "ds_read_b128 v[136:139], v204 offset:28672\n\t"
    "v_exp_f32 v7, v7\n\t"
    "v_mfma_f32_32x32x16_bf16 v[188:203], v[48:51], v[56:59], v[188:203]\n\t"
    "v_exp_f32 v8, v8\n\t"
    "ds_read_b128 v[80:83], v208 offset:16384\n\t"
    "buffer_load_dwordx4 v217, s[44:47], s53 offen offset:2048 sc1 lds\n\t"
    "s_add_u32 s53, s53, 2048\n\t"
    "v_exp_f32 v9, v9\n\t"
    "ds_read_b128 v[72:75], v204 offset:24576\n\t"
    "v_exp_f32 v10, v10\n\t"
    "v_smfmac_f32_32x32x32_bf16 v[156:171], v[140:143], v[0:7], v219\n\t"
    "v_exp_f32 v11, v11\n\t"
    "ds_read_b128 v[76:79], v204 offset:25600\n\t"
    "s_add_u32 m0, s56, 0\n\t"
    "v_exp_f32 v12, v12\n\t"
    "ds_read_b128 v[92:95], v209 offset:16384\n\t"
    "buffer_load_dwordx4 v214, s[40:43], s52 offen offset:2048 nt lds\n\t"
    "v_exp_f32 v13, v13\n\t"
    "v_mfma_f32_32x32x16_bf16 v[238:253], v[52:55], v[56:59], v[238:253]\n\t"
    "v_exp_f32 v14, v14\n\t"
    "ds_read_b128 v[84:87], v204 offset:26624\n\t"
    "v_exp_f32 v15, v15\n\t"
    "ds_read_b128 v[88:91], v204 offset:27648\n\t"
    "v_exp_f32 v16, v16\n\t"
    "buffer_load_dwordx4 v215, s[40:43], s52 offen offset:3072 nt lds\n\t"
    "s_add_u32 s52, s52, 128\n\t"
    "v_exp_f32 v17, v17\n\t"
    "v_smfmac_f32_32x32x32_bf16 v[156:171], v[144:147], v[8:15], v219\n\t"
    "v_exp_f32 v18, v18\n\t"
    "v_exp_f32 v19, v19\n\t"
    "s_waitcnt lgkmcnt(6)\n\t"
    "v_exp_f32 v20, v20\n\t"
    "v_mfma_f32_32x32x16_bf16 v[0:15], v[136:139], v[128:131], v[96:111]\n\t"
    "v_exp_f32 v21, v21\n\t"
    "s_add_u32 m0, s58, 12288\n\t"
    "v_exp_f32 v22, v22\n\t"
    "buffer_load_dwordx4 v218, s[48:51], s54 offen sc1 lds\n\t"
    "s_add_u32 s54, s54, 2048\n\t"
    "v_exp_f32 v23, v23\n\t"
    "v_mfma_f32_32x32x16_bf16 v[188:203], v[60:63], v[68:71], v[188:203]\n\t"
    "v_exp_f32 v24, v24\n\t"
    "v_exp_f32 v25, v25\n\t"
    "v_exp_f32 v26, v26\n\t"
    "v_smfmac_f32_32x32x32_bf16 v[172:187], v[140:143], v[16:23], v219\n\t"
    "v_exp_f32 v27, v27\n\t"
    "v_exp_f32 v28, v28\n\t"
    "v_exp_f32 v29, v29\n\t"
    "v_mfma_f32_32x32x16_bf16 v[238:253], v[64:67], v[68:71], v[238:253]\n\t"
    "v_exp_f32 v30, v30\n\t"
    "v_exp_f32 v31, v31\n\t"
    "s_waitcnt vmcnt(8) lgkmcnt(0)\n\t"
    "s_barrier\n\t"
    "v_exp_f32 v0, v0\n\t"
    "v_exp_f32 v1, v1\n\t"
    "v_smfmac_f32_32x32x32_bf16 v[172:187], v[144:147], v[24:31], v219\n\t"
    "s_add_u32 m0, s57, 12288\n\t"
    "v_exp_f32 v2, v2\n\t"
    "buffer_load_dwordx4 v216, s[44:47], s53 offen sc1 lds\n\t"
    "v_exp_f32 v3, v3\n\t"
    "s_mov_b64 exec, s[60:61]\n\t"
    "ds_read_b128 v[140:143], v205 offset:36864\n\t"
    "ds_read_b128 v[144:147], v205 offset:37376\n\t"
    "s_mov_b64 exec, -1\n\t"
    "v_exp_f32 v4, v4\n\t"
    "v_mfma_f32_32x32x16_bf16 v[16:31], v[136:139], v[132:135], v[112:127]\n\t"
    "v_exp_f32 v5, v5\n\t"
    "v_exp_f32 v6, v6\n\t"
    "ds_read_b128 v[136:139], v204 offset:40960\n\t"
    "v_exp_f32 v7, v7\n\t"
    "v_mfma_f32_32x32x16_bf16 v[188:203], v[72:75], v[80:83], v[188:203]\n\t"
    "v_exp_f32 v8, v8\n\t"
    "ds_read_b128 v[56:59], v206 offset:32768\n\t"
    "buffer_load_dwordx4 v217, s[44:47], s53 offen offset:2048 sc1 lds\n\t"
    "s_add_u32 s53, s53, 2048\n\t"
    "v_exp_f32 v9, v9\n\t"
    "ds_read_b128 v[48:51], v204 offset:36864\n\t"
    "v_exp_f32 v10, v10\n\t"
    "v_smfmac_f32_32x32x32_bf16 v[156:171], v[148:151], v[0:7], v219\n\t"
    "v_exp_f32 v11, v11\n\t"
    "ds_read_b128 v[52:55], v204 offset:37888\n\t"
    "s_add_u32 m0, s56, 16384\n\t"
    "v_exp_f32 v12, v12\n\t"
    "ds_read_b128 v[68:71], v207 offset:32768\n\t"
    "buffer_load_dwordx4 v212, s[40:43], s52 offen offset:0 nt lds\n\t"
    "v_exp_f32 v13, v13\n\t"
    "v_mfma_f32_32x32x16_bf16 v[238:253], v[76:79], v[80:83], v[238:253]\n\t"
    "v_exp_f32 v14, v14\n\t"
    "ds_read_b128 v[60:63], v204 offset:38912\n\t"
    "v_exp_f32 v15, v15\n\t"
    "ds_read_b128 v[64:67], v204 offset:39936\n\t"
    "v_exp_f32 v16, v16\n\t"
    "buffer_load_dwordx4 v213, s[40:43], s52 offen offset:1024 nt lds\n\t"
    "v_exp_f32 v17, v17\n\t"
    "v_smfmac_f32_32x32x32_bf16 v[156:171], v[152:155], v[8:15], v219\n\t"
    "v_exp_f32 v18, v18\n\t"
    "v_exp_f32 v19, v19\n\t"
    "s_waitcnt lgkmcnt(6)\n\t"
    "v_exp_f32 v20, v20\n\t"
    "v_mfma_f32_32x32x16_bf16 v[0:15], v[136:139], v[128:131], v[96:111]\n\t"
    "v_exp_f32 v21, v21\n\t"
    "s_add_u32 m0, s58, 24576\n\t"
    "v_exp_f32 v22, v22\n\t"
    "buffer_load_dwordx4 v218, s[48:51], s54 offen sc1 lds\n\t"
    "s_add_u32 s54, s54, 2048\n\t"
    "v_exp_f32 v23, v23\n\t"
    "v_mfma_f32_32x32x16_bf16 v[188:203], v[84:87], v[92:95], v[188:203]\n\t"
    "v_exp_f32 v24, v24\n\t"
    "v_exp_f32 v25, v25\n\t"
    "v_exp_f32 v26, v26\n\t"
    "v_smfmac_f32_32x32x32_bf16 v[172:187], v[148:151], v[16:23], v219\n\t"
    "v_exp_f32 v27, v27\n\t"
    "v_exp_f32 v28, v28\n\t"
    "v_exp_f32 v29, v29\n\t"
    "v_mfma_f32_32x32x16_bf16 v[238:253], v[88:91], v[92:95], v[238:253]\n\t"
    "v_exp_f32 v30, v30\n\t"
    "v_exp_f32 v31, v31\n\t"
    "s_waitcnt vmcnt(8) lgkmcnt(0)\n\t"
    "s_barrier\n\t"
    "v_exp_f32 v0, v0\n\t"
    "v_exp_f32 v1, v1\n\t"
    "v_smfmac_f32_32x32x32_bf16 v[172:187], v[152:155], v[24:31], v219\n\t"
    "s_add_u32 m0, s57, 24576\n\t"
    "v_exp_f32 v2, v2\n\t"
    "buffer_load_dwordx4 v216, s[44:47], s53 offen sc1 lds\n\t"
    "v_exp_f32 v3, v3\n\t"
    "s_mov_b64 exec, s[60:61]\n\t"
    "ds_read_b128 v[148:151], v205 offset:0\n\t"
    "ds_read_b128 v[152:155], v205 offset:512\n\t"
    "s_mov_b64 exec, -1\n\t"
    "v_exp_f32 v4, v4\n\t"
    "v_mfma_f32_32x32x16_bf16 v[16:31], v[136:139], v[132:135], v[112:127]\n\t"
    "v_exp_f32 v5, v5\n\t"
    "v_exp_f32 v6, v6\n\t"
    "ds_read_b128 v[136:139], v204 offset:4096\n\t"
    "v_exp_f32 v7, v7\n\t"
    "v_mfma_f32_32x32x16_bf16 v[188:203], v[48:51], v[56:59], v[188:203]\n\t"
    "v_exp_f32 v8, v8\n\t"
    "ds_read_b128 v[80:83], v208 offset:32768\n\t"
    "buffer_load_dwordx4 v217, s[44:47], s53 offen offset:2048 sc1 lds\n\t"
    "s_add_u32 s53, s53, 2048\n\t"
    "v_exp_f32 v9, v9\n\t"
    "ds_read_b128 v[72:75], v204 offset:0\n\t"
    "v_exp_f32 v10, v10\n\t"
    "v_smfmac_f32_32x32x32_bf16 v[156:171], v[140:143], v[0:7], v219\n\t"
    "v_exp_f32 v11, v11\n\t"
    "ds_read_b128 v[76:79], v204 offset:1024\n\t"
    "s_add_u32 m0, s56, 16384\n\t"
    "v_exp_f32 v12, v12\n\t"
    "ds_read_b128 v[92:95], v209 offset:32768\n\t"
    "buffer_load_dwordx4 v214, s[40:43], s52 offen offset:2048 nt lds\n\t"
    "v_exp_f32 v13, v13\n\t"
    "v_mfma_f32_32x32x16_bf16 v[238:253], v[52:55], v[56:59], v[238:253]\n\t"
    "v_exp_f32 v14, v14\n\t"
    "ds_read_b128 v[84:87], v204 offset:2048\n\t"
    "v_exp_f32 v15, v15\n\t"
    "ds_read_b128 v[88:91], v204 offset:3072\n\t"
    "v_exp_f32 v16, v16\n\t"
    "buffer_load_dwordx4 v215, s[40:43], s52 offen offset:3072 nt lds\n\t"
    "s_add_u32 s52, s52, 128\n\t"
    "v_exp_f32 v17, v17\n\t"
    "v_smfmac_f32_32x32x32_bf16 v[156:171], v[144:147], v[8:15], v219\n\t"
    "v_exp_f32 v18, v18\n\t"
    "v_exp_f32 v19, v19\n\t"
    "s_waitcnt lgkmcnt(6)\n\t"
    "v_exp_f32 v20, v20\n\t"
    "v_mfma_f32_32x32x16_bf16 v[0:15], v[136:139], v[128:131], v[96:111]\n\t"
    "v_exp_f32 v21, v21\n\t"
    "s_add_u32 m0, s58, 36864\n\t"
    "v_exp_f32 v22, v22\n\t"
    "buffer_load_dwordx4 v218, s[48:51], s54 offen sc1 lds\n\t"
    "s_add_u32 s54, s54, 2048\n\t"
    "v_exp_f32 v23, v23\n\t"
    "v_mfma_f32_32x32x16_bf16 v[188:203], v[60:63], v[68:71], v[188:203]\n\t"
    "v_exp_f32 v24, v24\n\t"
    "v_exp_f32 v25, v25\n\t"
    "v_exp_f32 v26, v26\n\t"
    "v_smfmac_f32_32x32x32_bf16 v[172:187], v[140:143], v[16:23], v219\n\t"
    "v_exp_f32 v27, v27\n\t"
    "v_exp_f32 v28, v28\n\t"
    "v_exp_f32 v29, v29\n\t"
    "v_mfma_f32_32x32x16_bf16 v[238:253], v[64:67], v[68:71], v[238:253]\n\t"
    "v_exp_f32 v30, v30\n\t"
    "v_exp_f32 v31, v31\n\t"
    "s_waitcnt vmcnt(8) lgkmcnt(0)\n\t"
    "s_barrier\n\t"
    "v_exp_f32 v0, v0\n\t"
    "v_exp_f32 v1, v1\n\t"
    "v_smfmac_f32_32x32x32_bf16 v[172:187], v[144:147], v[24:31], v219\n\t"
    "s_add_u32 m0, s57, 36864\n\t"
    "v_exp_f32 v2, v2\n\t"
    "buffer_load_dwordx4 v216, s[44:47], s53 offen sc1 lds\n\t"
    "v_exp_f32 v3, v3\n\t"
    "s_mov_b64 exec, s[60:61]\n\t"
    "ds_read_b128 v[140:143], v205 offset:12288\n\t"
    "ds_read_b128 v[144:147], v205 offset:12800\n\t"
    "s_mov_b64 exec, -1\n\t"
    "v_exp_f32 v4, v4\n\t"
    "v_mfma_f32_32x32x16_bf16 v[16:31], v[136:139], v[132:135], v[112:127]\n\t"
    "v_exp_f32 v5, v5\n\t"
    "v_exp_f32 v6, v6\n\t"
    "ds_read_b128 v[136:139], v204 offset:16384\n\t"
    "v_exp_f32 v7, v7\n\t"
    "v_mfma_f32_32x32x16_bf16 v[188:203], v[72:75], v[80:83], v[188:203]\n\t"
    "v_exp_f32 v8, v8\n\t"
    "ds_read_b128 v[56:59], v206 offset:0\n\t"
    "buffer_load_dwordx4 v217, s[44:47], s53 offen offset:2048 sc1 lds\n\t"
    "s_add_u32 s53, s53, 2048\n\t"
    "v_exp_f32 v9, v9\n\t"
    "ds_read_b128 v[48:51], v204 offset:12288\n\t"
    "v_exp_f32 v10, v10\n\t"
    "v_smfmac_f32_32x32x32_bf16 v[156:171], v[148:151], v[0:7], v219\n\t"
    "v_exp_f32 v11, v11\n\t"
    "ds_read_b128 v[52:55], v204 offset:13312\n\t"
    "s_add_u32 m0, s56, 32768\n\t"
    "v_exp_f32 v12, v12\n\t"
    "ds_read_b128 v[68:71], v207 offset:0\n\t"
    "buffer_load_dwordx4 v212, s[40:43], s52 offen offset:0 nt lds\n\t"
    "v_exp_f32 v13, v13\n\t"
    "v_mfma_f32_32x32x16_bf16 v[238:253], v[76:79], v[80:83], v[238:253]\n\t"
    "v_exp_f32 v14, v14\n\t"
    "ds_read_b128 v[60:63], v204 offset:14336\n\t"
    "v_exp_f32 v15, v15\n\t"
    "ds_read_b128 v[64:67], v204 offset:15360\n\t"
    "v_exp_f32 v16, v16\n\t"
    "buffer_load_dwordx4 v213, s[40:43], s52 offen offset:1024 nt lds\n\t"
    "v_exp_f32 v17, v17\n\t"
    "v_smfmac_f32_32x32x32_bf16 v[156:171], v[152:155], v[8:15], v219\n\t"
    "v_exp_f32 v18, v18\n\t"
    "v_exp_f32 v19, v19\n\t"
    "s_waitcnt lgkmcnt(6)\n\t"
    "v_exp_f32 v20, v20\n\t"
    "v_mfma_f32_32x32x16_bf16 v[0:15], v[136:139], v[128:131], v[96:111]\n\t"
    "v_exp_f32 v21, v21\n\t"
    "s_add_u32 m0, s58, 0\n\t"
    "v_exp_f32 v22, v22\n\t"
    "buffer_load_dwordx4 v218, s[48:51], s54 offen sc1 lds\n\t"
    "s_add_u32 s54, s54, 2048\n\t"
    "v_exp_f32 v23, v23\n\t"
    "v_mfma_f32_32x32x16_bf16 v[188:203], v[84:87], v[92:95], v[188:203]\n\t"
    "v_exp_f32 v24, v24\n\t"
    "v_exp_f32 v25, v25\n\t"
    "v_exp_f32 v26, v26\n\t"
    "v_smfmac_f32_32x32x32_bf16 v[172:187], v[148:151], v[16:23], v219\n\t"
    "v_exp_f32 v27, v27\n\t"
    "v_exp_f32 v28, v28\n\t"
    "v_exp_f32 v29, v29\n\t"
    "v_mfma_f32_32x32x16_bf16 v[238:253], v[88:91], v[92:95], v[238:253]\n\t"
    "v_exp_f32 v30, v30\n\t"
    "v_exp_f32 v31, v31\n\t"
    "s_sub_u32 s59, s59, 1\n\t"
    "s_cmp_lg_u32 s59, 0\n\t"
    "s_waitcnt vmcnt(8) lgkmcnt(0)\n\t"
    "s_cbranch_scc1 2b\n\t"
    "s_setprio 0\n\t"
    "v_smfmac_f32_32x32x32_bf16 v[172:187], v[152:155], v[24:31], v219\n\t"
    "ds_write_b128 v222, v[156:159]\n\t"
    "ds_write_b128 v222, v[160:163] offset:1024\n\t"
    "ds_write_b32 v223, v164\n\t"
    "s_nop 8\n\t"
    "ds_write_b128 v222, v[172:175] offset:2048\n\t"
    "ds_write_b128 v222, v[176:179] offset:3072\n\t"
    "ds_write_b32 v223, v180 offset:256\n\t"
    "s_waitcnt lgkmcnt(0)\n\t"
    "s_barrier\n\t"
    "v_mfma_f32_32x32x16_bf16 v[188:203], v[48:51], v[56:59], v[188:203]\n\t"
    "ds_read_b128 v[80:83], v208 offset:0\n\t"
    "s_add_u32 m0, s57, 0\n\t"
    "s_nop 0\n\t"
    "buffer_load_dwordx4 v216, s[44:47], s53 offen sc1 lds\n\t"
    "v_mfma_f32_32x32x16_bf16 v[238:253], v[52:55], v[56:59], v[238:253]\n\t"
    "ds_read_b128 v[72:75], v204 offset:24576\n\t"
    "ds_read_b128 v[76:79], v204 offset:25600\n\t"
    "buffer_load_dwordx4 v217, s[44:47], s53 offen offset:2048 sc1 lds\n\t"
    "s_add_u32 s53, s53, 2048\n\t"
    "s_add_u32 m0, s57, 12288\n\t"
    "s_nop 0\n\t"
    "buffer_load_dwordx4 v216, s[44:47], s53 offen sc1 lds\n\t"
    "v_mfma_f32_32x32x16_bf16 v[188:203], v[60:63], v[68:71], v[188:203]\n\t"
    "ds_read_b128 v[92:95], v209 offset:0\n\t"
    "buffer_load_dwordx4 v217, s[44:47], s53 offen offset:2048 sc1 lds\n\t"
    "s_add_u32 s53, s53, 2048\n\t"
    "v_mfma_f32_32x32x16_bf16 v[238:253], v[64:67], v[68:71], v[238:253]\n\t"
    "ds_read_b128 v[84:87], v204 offset:26624\n\t"
    "ds_read_b128 v[88:91], v204 offset:27648\n\t"
    "s_add_u32 m0, s56, 32768\n\t"
    "s_nop 0\n\t"
    "buffer_load_dwordx4 v214, s[40:43], s52 offen offset:2048 nt lds\n\t"
    "buffer_load_dwordx4 v215, s[40:43], s52 offen offset:3072 nt lds\n\t"
    "s_add_u32 s52, s52, 128\n\t"
    "s_waitcnt vmcnt(9) lgkmcnt(0)\n\t"
    "s_barrier\n\t"
    "v_mfma_f32_32x32x16_bf16 v[188:203], v[72:75], v[80:83], v[188:203]\n\t"
    "ds_read_b128 v[56:59], v206 offset:16384\n\t"
    "s_add_u32 m0, s57, 24576\n\t"
    "s_nop 0\n\t"
    "buffer_load_dwordx4 v216, s[44:47], s53 offen sc1 lds\n\t"
    "v_mfma_f32_32x32x16_bf16 v[238:253], v[76:79], v[80:83], v[238:253]\n\t"
    "ds_read_b128 v[48:51], v204 offset:36864\n\t"
    "ds_read_b128 v[52:55], v204 offset:37888\n\t"
    "buffer_load_dwordx4 v217, s[44:47], s53 offen offset:2048 sc1 lds\n\t"
    "s_add_u32 s53, s53, 2048\n\t"
    "v_mfma_f32_32x32x16_bf16 v[188:203], v[84:87], v[92:95], v[188:203]\n\t"
    "ds_read_b128 v[68:71], v207 offset:16384\n\t"
    "s_add_u32 m0, s56, 0\n\t"
    "s_nop 0\n\t"
    "buffer_load_dwordx4 v212, s[40:43], s52 offen offset:0 nt lds\n\t"
    "v_mfma_f32_32x32x16_bf16 v[238:253], v[88:91], v[92:95], v[238:253]\n\t"
    "ds_read_b128 v[60:63], v204 offset:38912\n\t"
    "ds_read_b128 v[64:67], v204 offset:39936\n\t"
    "buffer_load_dwordx4 v213, s[40:43], s52 offen offset:1024 nt lds\n\t"
    "s_waitcnt vmcnt(8) lgkmcnt(0)\n\t"
    "s_barrier\n\t"
    "v_mfma_f32_32x32x16_bf16 v[188:203], v[48:51], v[56:59], v[188:203]\n\t"
    "ds_read_b128 v[80:83], v208 offset:16384\n\t"
    "s_add_u32 m0, s57, 36864\n\t"
    "s_nop 0\n\t"
    "buffer_load_dwordx4 v216, s[44:47], s53 offen sc1 lds\n\t"
    "v_mfma_f32_32x32x16_bf16 v[238:253], v[52:55], v[56:59], v[238:253]\n\t"
    "ds_read_b128 v[72:75], v204 offset:0\n\t"
    "ds_read_b128 v[76:79], v204 offset:1024\n\t"
    "buffer_load_dwordx4 v217, s[44:47], s53 offen offset:2048 sc1 lds\n\t"
    "s_add_u32 s53, s53, 2048\n\t"
    "v_mfma_f32_32x32x16_bf16 v[188:203], v[60:63], v[68:71], v[188:203]\n\t"
    "ds_read_b128 v[92:95], v209 offset:16384\n\t"
    "s_add_u32 m0, s56, 0\n\t"
    "s_nop 0\n\t"
    "buffer_load_dwordx4 v214, s[40:43], s52 offen offset:2048 nt lds\n\t"
    "v_mfma_f32_32x32x16_bf16 v[238:253], v[64:67], v[68:71], v[238:253]\n\t"
    "ds_read_b128 v[84:87], v204 offset:2048\n\t"
    "ds_read_b128 v[88:91], v204 offset:3072\n\t"
    "buffer_load_dwordx4 v215, s[40:43], s52 offen offset:3072 nt lds\n\t"
    "s_add_u32 s52, s52, 128\n\t"
    "s_waitcnt vmcnt(8) lgkmcnt(0)\n\t"
    "s_barrier\n\t"
    "v_mfma_f32_32x32x16_bf16 v[188:203], v[72:75], v[80:83], v[188:203]\n\t"
    "ds_read_b128 v[56:59], v206 offset:32768\n\t"
    "s_add_u32 m0, s57, 0\n\t"
    "s_nop 0\n\t"
    "buffer_load_dwordx4 v216, s[44:47], s53 offen sc1 lds\n\t"
    "v_mfma_f32_32x32x16_bf16 v[238:253], v[76:79], v[80:83], v[238:253]\n\t"
    "ds_read_b128 v[48:51], v204 offset:12288\n\t"
    "ds_read_b128 v[52:55], v204 offset:13312\n\t"
    "buffer_load_dwordx4 v217, s[44:47], s53 offen offset:2048 sc1 lds\n\t"
    "s_add_u32 s53, s53, 2048\n\t"
    "s_add_u32 m0, s83, 0\n\t"
    "s_nop 0\n\t"
    "buffer_load_dwordx4 v232, s[84:87], 0 offen lds\n\t"
    "v_mfma_f32_32x32x16_bf16 v[188:203], v[84:87], v[92:95], v[188:203]\n\t"
    "ds_read_b128 v[68:71], v207 offset:32768\n\t"
    "buffer_load_dwordx4 v233, s[84:87], 0 offen offset:1024 lds\n\t"
    "v_mfma_f32_32x32x16_bf16 v[238:253], v[88:91], v[92:95], v[238:253]\n\t"
    "ds_read_b128 v[60:63], v204 offset:14336\n\t"
    "ds_read_b128 v[64:67], v204 offset:15360\n\t"
    "buffer_load_dwordx4 v234, s[84:87], 0 offen offset:2048 lds\n\t"
    "buffer_load_dwordx4 v235, s[84:87], 0 offen offset:3072 lds\n\t"
    "s_waitcnt vmcnt(12) lgkmcnt(0)\n\t"
    "s_barrier\n\t"
    "v_mfma_f32_32x32x16_bf16 v[188:203], v[48:51], v[56:59], v[188:203]\n\t"
    "ds_read_b128 v[80:83], v208 offset:32768\n\t"
    "v_mfma_f32_32x32x16_bf16 v[238:253], v[52:55], v[56:59], v[238:253]\n\t"
    "ds_read_b128 v[72:75], v204 offset:24576\n\t"
    "ds_read_b128 v[76:79], v204 offset:25600\n\t"
    "v_mfma_f32_32x32x16_bf16 v[188:203], v[60:63], v[68:71], v[188:203]\n\t"
    "ds_read_b128 v[92:95], v209 offset:32768\n\t"
    "v_mfma_f32_32x32x16_bf16 v[238:253], v[64:67], v[68:71], v[238:253]\n\t"
    "ds_read_b128 v[84:87], v204 offset:26624\n\t"
    "ds_read_b128 v[88:91], v204 offset:27648\n\t"
    "s_waitcnt vmcnt(6) lgkmcnt(0)\n\t"
    "s_barrier\n\t"
    "global_load_dwordx2 v[228:229], v226, s[62:63] sc1\n\t"
    "v_mfma_f32_32x32x16_bf16 v[188:203], v[72:75], v[80:83], v[188:203]\n\t"
    "ds_read_b128 v[56:59], v206 offset:0\n\t"
    "v_mfma_f32_32x32x16_bf16 v[238:253], v[76:79], v[80:83], v[238:253]\n\t"
    "ds_read_b128 v[48:51], v204 offset:36864\n\t"
    "ds_read_b128 v[52:55], v204 offset:37888\n\t"
    "v_mfma_f32_32x32x16_bf16 v[188:203], v[84:87], v[92:95], v[188:203]\n\t"
    "ds_read_b128 v[68:71], v207 offset:0\n\t"
    "v_mfma_f32_32x32x16_bf16 v[238:253], v[88:91], v[92:95], v[238:253]\n\t"
    "ds_read_b128 v[60:63], v204 offset:38912\n\t"
    "ds_read_b128 v[64:67], v204 offset:39936\n\t"
    "s_waitcnt vmcnt(4) lgkmcnt(0)\n\t"
    "s_barrier\n\t"
    "v_mfma_f32_32x32x16_bf16 v[188:203], v[48:51], v[56:59], v[188:203]\n\t"
    "ds_read_b128 v[80:83], v208 offset:0\n\t"
    "v_mfma_f32_32x32x16_bf16 v[238:253], v[52:55], v[56:59], v[238:253]\n\t"
    "ds_read_b128 v[72:75], v204 offset:0\n\t"
    "ds_read_b128 v[76:79], v204 offset:1024\n\t"
    "v_mfma_f32_32x32x16_bf16 v[188:203], v[60:63], v[68:71], v[188:203]\n\t"
    "ds_read_b128 v[92:95], v209 offset:0\n\t"
    "v_mfma_f32_32x32x16_bf16 v[238:253], v[64:67], v[68:71], v[238:253]\n\t"
    "ds_read_b128 v[84:87], v204 offset:2048\n\t"
    "ds_read_b128 v[88:91], v204 offset:3072\n\t"
    "s_waitcnt vmcnt(1) lgkmcnt(0)\n\t"
    "s_barrier\n\t"
    "v_mfma_f32_32x32x16_bf16 v[188:203], v[72:75], v[80:83], v[188:203]\n\t"
    "v_mfma_f32_32x32x16_bf16 v[238:253], v[76:79], v[80:83], v[238:253]\n\t"
    "v_mfma_f32_32x32x16_bf16 v[188:203], v[84:87], v[92:95], v[188:203]\n\t"
    "v_mfma_f32_32x32x16_bf16 v[238:253], v[88:91], v[92:95], v[238:253]\n\t"
    "s_waitcnt vmcnt(0)\n\t"
    "v_cmp_ne_u64 vcc, v[228:229], v[230:231]\n\t"
    "s_nop 4\n\t"
    "s_or_b32 s75, vcc_lo, vcc_hi\n\t"
    "s_nop 3\n\t"
        : P2_HELD, "+{s[48:51]}"(skv), "+{s52}"(soc), "+{s53}"(soab), "+{s54}"(sokv), "={s75}"(late) ST_OUT ST_OUT2
        : "{s[40:43]}"(sc), "{s[44:47]}"(sab), "{s56}"(m0c), "{s57}"(m0ab), "{s58}"(m0kv), "{s60}"(m1), "{s61}"(m2), P2_READS, P2_WO,
          "{v[212:215]}"(vc), "{v[216:217]}"(vab), "{v218}"(vkv), "{v[222:223]}"(xa), "{v226}"(fwait), "{s[62:63]}"(fw), "{v[230:231]}"(tag)
        : "memory", "scc", "vcc", "s59", "v228", "v229", P2_CLOBBER, "v52", "v53", "v54", "v55", "v56", "v57", "v58", "v59", "v60", "v61", "v62", "v63", "v64", "v65", "v66", "v67", "v68", "v69", "v70", "v71", "v72", "v73", "v74", "v75", "v76", "v77", "v78", "v79", "v80", "v81", "v82", "v83", "v84", "v85", "v86", "v87", "v88", "v89", "v90", "v91", "v92", "v93", "v94", "v95");
  } else {
    const int lane = lane0, r = lane & 31, h = lane >> 5, rr = lane >> 3, pc = lane & 7;
    u32x4 sx, sw;
    unsigned soff, s53;
    {
      const int mt = 8 * (blockIdx.x & 1) + ((blockIdx.x >> 3) & 7), nt = 4 * ((blockIdx.x >> 1) & 3) + (blockIdx.x >> 6);
      sx = srd(x + (size_t)mt * 131072, 131072); sw = srd(wqkv + nt * 64, 1572864 - nt * 64);
      soff = (P1ROT & 7) * 128;
    }
    const unsigned m0x = l0 + qs * 4096, m0w = l0 + 16384 + qs * 3072;
    const unsigned xa0 = (32 * qs + rr) * 1024 + ((pc ^ (rr >> 1)) * 16), xa1 = (32 * qs + 8 + rr) * 1024 + ((pc ^ ((rr >> 1) + 4)) * 16) - 1024;
    const u32x4 vx = {xa0, xa1, xa0 + 16384 - 2048, xa1 + 16384 - 2048};
    const unsigned vw = (16 * qs + (lane >> 2)) * 3072 + (lane & 3) * 16;
    asm volatile(
    HZ
    "s_nop 4\n\t"
    "s_mul_i32 s53, s52, 1536\n\t"
    "s_add_u32 m0, s55, 0\n\t"
    "s_nop 0\n\t"
    "buffer_load_dwordx4 v112, s[40:43], s52 offen lds\n\t"
    "buffer_load_dwordx4 v113, s[40:43], s52 offen offset:1024 lds\n\t"
    "buffer_load_dwordx4 v114, s[40:43], s52 offen offset:2048 lds\n\t"
    "buffer_load_dwordx4 v115, s[40:43], s52 offen offset:3072 lds\n\t"
    "s_add_u32 m0, s56, 0\n\t"
    "s_nop 0\n\t"
    "buffer_load_dwordx4 v116, s[44:47], s53 offen lds\n\t"
    "buffer_load_dwordx4 v116, s[44:47], s53 offen offset:1024 lds\n\t"
    "buffer_load_dwordx4 v116, s[44:47], s53 offen offset:2048 lds\n\t"
    "s_add_u32 s52, s52, 128\n\t"
    "s_and_b32 s52, s52, 0x3ff\n\t"
    "s_mul_i32 s53, s52, 1536\n\t"
    "s_mov_b32 m0, s65\n\t"
    "s_nop 0\n\t"
    "global_load_lds_dwordx4 v125, s[74:75]\n\t"
    "s_add_u32 m0, s55, 28672\n\t"
    "s_nop 0\n\t"
    "buffer_load_dwordx4 v112, s[40:43], s52 offen lds\n\t"
    "buffer_load_dwordx4 v113, s[40:43], s52 offen offset:1024 lds\n\t"
    "buffer_load_dwordx4 v114, s[40:43], s52 offen offset:2048 lds\n\t"
    "buffer_load_dwordx4 v115, s[40:43], s52 offen offset:3072 lds\n\t"
    "s_add_u32 m0, s56, 28672\n\t"
    "s_nop 0\n\t"
    "buffer_load_dwordx4 v116, s[44:47], s53 offen lds\n\t"
    "buffer_load_dwordx4 v116, s[44:47], s53 offen offset:1024 lds\n\t"
    "buffer_load_dwordx4 v116, s[44:47], s53 offen offset:2048 lds\n\t"
    "s_add_u32 s52, s52, 128\n\t"
    "s_and_b32 s52, s52, 0x3ff\n\t"
    "s_mul_i32 s53, s52, 1536\n\t"
    "s_add_u32 m0, s55, 57344\n\t"
    "s_nop 0\n\t"
    "buffer_load_dwordx4 v112, s[40:43], s52 offen lds\n\t"
    "buffer_load_dwordx4 v113, s[40:43], s52 offen offset:1024 lds\n\t"
    "buffer_load_dwordx4 v114, s[40:43], s52 offen offset:2048 lds\n\t"
    "buffer_load_dwordx4 v115, s[40:43], s52 offen offset:3072 lds\n\t"
    "s_add_u32 m0, s56, 57344\n\t"
    "s_nop 0\n\t"
    "buffer_load_dwordx4 v116, s[44:47], s53 offen lds\n\t"
    "buffer_load_dwordx4 v116, s[44:47], s53 offen offset:1024 lds\n\t"
    "buffer_load_dwordx4 v116, s[44:47], s53 offen offset:2048 lds\n\t"
    "s_add_u32 s52, s52, 128\n\t"
    "s_and_b32 s52, s52, 0x3ff\n\t"
    "s_mul_i32 s53, s52, 1536\n\t"
        : "+{s52}"(soff), "={s53}"(s53)
        : "{s[40:43]}"(sx), "{s[44:47]}"(sw), "{s55}"(m0x), "{s56}"(m0w), "{s65}"(l0 + LDS_GW), "{s[74:75]}"(gate_w), "{v[112:115]}"(vx),
          "{v116}"(vw), "{v125}"(lane * 16)
        : "memory", "scc");
    ST(13);
    TILE_INDICES(late_block())
    P1_READ_ADDRESSES
    const u32x4 sd = srd((nt < 8 ? dre : dim) + (size_t)(nt & 7) * 65536, 65536);
    const unsigned d0 = (16 * qs + rr) * 1024 + ((pc ^ (rr >> 1)) * 16), d1 = (16 * qs + 8 + rr) * 1024 + ((pc ^ ((rr >> 1) + 4)) * 16) - 1024;
    unsigned z;
    P2_READ_ADDRESSES
    u64 here; unsigned skip, len;
    asm volatile("s_getpc_b64 %0\nfused_code_mid:\n\ts_mov_b32 %1, fused_code_tail-fused_code_mid\n\ts_mov_b32 %2, fused_code_end-fused_code_tail"
                 : "=s"(here), "=s"(skip), "=s"(len));
    here = qs == 0 ? here + skip : qs == 1 ? (u64)cb : (u64)wo;
    u32x2 tail = {(unsigned)__builtin_amdgcn_readfirstlane((unsigned)here), (unsigned)__builtin_amdgcn_readfirstlane((unsigned)(here >> 32))};
    unsigned vcode = qs == 0 ? (lane * 128 < len ? lane * 128 : 0) : qs == 1 ? (32 * e4 + (lane >> 1)) * 4096 + (14 + (lane & 1)) * 128
                                                                              : (384 + 16 * (i >> 2) + 8 * (qs & 1) + (lane & 7)) * 1024 + eb * 128;
    unsigned pub = qs == 0 ? 2 : 0xffff;
    u32x2 fo = {(unsigned)((F_A + ABPOLL) * 8 * FS), (unsigned)((F_A + nt * 16 + mt) * 8 * FS)};
    const char* od = ws + WS_O + (size_t)(qt * 32 + head) * 1024;
    unsigned fo2 = (F_O + qb * 32 + head) * 8 * FS;
    const char* od2 = ws + WS_O + (size_t)((qt + 1) * 32 + head) * 1024;
    u32x2 xa = {l0 + LDS_XO + qs * 4096 + lane * 16, l0 + LDS_XL + qs * 512 + lane * 4};
    unsigned vl16 = lane * 16;
    asm volatile("" : "+{s[64:65]}"(od), "+{s[66:67]}"(od2), "+{v[222:223]}"(xa), "+{v224}"(vl16), "+v"(fo2), P2_PINS, "+{v218}"(vcode), "+{v[226:227]}"(fo), "+s"(tail), "+s"(pub));
    f32x16 av, ae;
    u32x16 fa, fb, fc;
    asm volatile(
    HZ
    "v_mov_b32 v0, 0\n\t"
    "v_mov_b32 v1, 0\n\t"
    "v_mov_b32 v2, 0\n\t"
    "v_mov_b32 v3, 0\n\t"
    "v_mov_b32 v4, 0\n\t"
    "v_mov_b32 v5, 0\n\t"
    "v_mov_b32 v6, 0\n\t"
    "v_mov_b32 v7, 0\n\t"
    "v_mov_b32 v8, 0\n\t"
    "v_mov_b32 v9, 0\n\t"
    "v_mov_b32 v10, 0\n\t"
    "v_mov_b32 v11, 0\n\t"
    "v_mov_b32 v12, 0\n\t"
    "v_mov_b32 v13, 0\n\t"
    "v_mov_b32 v14, 0\n\t"
    "v_mov_b32 v15, 0\n\t"
    "v_mov_b32 v32, 0\n\t"
    "v_mov_b32 v33, 0\n\t"
    "v_mov_b32 v34, 0\n\t"
    "v_mov_b32 v35, 0\n\t"
    "v_mov_b32 v36, 0\n\t"
    "v_mov_b32 v37, 0\n\t"
    "v_mov_b32 v38, 0\n\t"
    "v_mov_b32 v39, 0\n\t"
    "v_mov_b32 v40, 0\n\t"
    "v_mov_b32 v41, 0\n\t"
    "v_mov_b32 v42, 0\n\t"
    "v_mov_b32 v43, 0\n\t"
    "v_mov_b32 v44, 0\n\t"
    "v_mov_b32 v45, 0\n\t"
    "v_mov_b32 v46, 0\n\t"
    "v_mov_b32 v47, 0\n\t"
    "v_mov_b32 v72, 0\n\t"
    "v_mov_b32 v73, 0\n\t"
    "v_mov_b32 v74, 0\n\t"
    "v_mov_b32 v75, 0\n\t"
    "v_mov_b32 v76, 0\n\t"
    "v_mov_b32 v77, 0\n\t"
    "v_mov_b32 v78, 0\n\t"
    "v_mov_b32 v79, 0\n\t"
    "v_mov_b32 v80, 0\n\t"
    "v_mov_b32 v81, 0\n\t"
    "v_mov_b32 v82, 0\n\t"
    "v_mov_b32 v83, 0\n\t"
    "v_mov_b32 v84, 0\n\t"
    "v_mov_b32 v85, 0\n\t"
    "v_mov_b32 v86, 0\n\t"
    "v_mov_b32 v87, 0\n\t"
    "v_mov_b32 v88, 0\n\t"
    "v_mov_b32 v89, 0\n\t"
    "v_mov_b32 v90, 0\n\t"
    "v_mov_b32 v91, 0\n\t"
    "v_mov_b32 v92, 0\n\t"
    "v_mov_b32 v93, 0\n\t"
    "v_mov_b32 v94, 0\n\t"
    "v_mov_b32 v95, 0\n\t"
    "global_load_dwordx2 v[230:231], v0, s[72:73] sc1\n\t"
    "s_waitcnt vmcnt(14)\n\t"
    "s_barrier\n\t"
    "s_add_u32 m0, s55, 86016\n\t"
    "s_waitcnt lgkmcnt(3)\n\t"
    "v_mfma_f32_32x32x16_bf16 v[0:15], v[72:75], v[76:79], v[0:15]\n\t"
    "buffer_load_dwordx4 v112, s[40:43], s52 offen lds\n\t"
    "ds_read_b128 v[48:51], v100\n\t"
    "ds_read_b64_tr_b16 v[52:53], v104 offset:2048\n\t"
    "ds_read_b64_tr_b16 v[54:55], v104 offset:2304\n\t"
    "buffer_load_dwordx4 v113, s[40:43], s52 offen offset:1024 lds\n\t"
    "s_waitcnt lgkmcnt(3)\n\t"
    "v_mfma_f32_32x32x16_bf16 v[0:15], v[84:87], v[88:91], v[0:15]\n\t"
    "buffer_load_dwordx4 v114, s[40:43], s52 offen offset:2048 lds\n\t"
    "ds_read_b128 v[60:63], v101\n\t"
    "ds_read_b64_tr_b16 v[64:65], v104 offset:5120\n\t"
    "ds_read_b64_tr_b16 v[66:67], v104 offset:5376\n\t"
    "v_add_u32 v100, 28672, v100\n\t"
    "v_add_u32 v108, 28672, v108\n\t"
    "buffer_load_dwordx4 v115, s[40:43], s52 offen offset:3072 lds\n\t"
    "s_add_u32 m0, s56, 86016\n\t"
    "s_waitcnt lgkmcnt(3)\n\t"
    "v_mfma_f32_32x32x16_bf16 v[0:15], v[48:51], v[52:55], v[0:15]\n\t"
    "buffer_load_dwordx4 v116, s[44:47], s53 offen lds\n\t"
    "ds_read_b128 v[72:75], v102\n\t"
    "ds_read_b64_tr_b16 v[76:77], v104 offset:8192\n\t"
    "ds_read_b64_tr_b16 v[78:79], v104 offset:8448\n\t"
    "v_add_u32 v101, 28672, v101\n\t"
    "v_add_u32 v109, 28672, v109\n\t"
    "buffer_load_dwordx4 v116, s[44:47], s53 offen offset:1024 lds\n\t"
    "s_waitcnt lgkmcnt(3)\n\t"
    "v_mfma_f32_32x32x16_bf16 v[0:15], v[60:63], v[64:67], v[0:15]\n\t"
    "buffer_load_dwordx4 v116, s[44:47], s53 offen offset:2048 lds\n\t"
    "s_add_u32 s52, s52, 128\n\t"
    "s_and_b32 s52, s52, 0x3ff\n\t"
    "s_mul_i32 s53, s52, 1536\n\t"
    "ds_read_b128 v[84:87], v103\n\t"
    "ds_read_b64_tr_b16 v[88:89], v104 offset:11264\n\t"
    "ds_read_b64_tr_b16 v[90:91], v104 offset:11520\n\t"
    "v_add_u32 v102, 28672, v102\n\t"
    "v_add_u32 v110, 28672, v110\n\t"
    "v_add_u32 v103, 28672, v103\n\t"
    "v_add_u32 v111, 28672, v111\n\t"
    "v_add_u32 v104, 28672, v104\n\t"
    "s_cmp_eq_u32 s62, 0\n\t"
    "s_cbranch_scc1 40f\n\t"
    "30:\n\t"
    "s_waitcnt vmcnt(15)\n\t"
    "s_barrier\n\t"
    "s_add_u32 m0, s55, 114688\n\t"
    "s_waitcnt lgkmcnt(3)\n\t"
    "v_mfma_f32_32x32x16_bf16 v[0:15], v[72:75], v[76:79], v[0:15]\n\t"
    "buffer_load_dwordx4 v112, s[40:43], s52 offen lds\n\t"
    "ds_read_b128 v[48:51], v100\n\t"
    "ds_read_b64_tr_b16 v[52:53], v104 offset:2048\n\t"
    "ds_read_b64_tr_b16 v[54:55], v104 offset:2304\n\t"
    "buffer_load_dwordx4 v113, s[40:43], s52 offen offset:1024 lds\n\t"
    "s_waitcnt lgkmcnt(3)\n\t"
    "v_mfma_f32_32x32x16_bf16 v[0:15], v[84:87], v[88:91], v[0:15]\n\t"
    "buffer_load_dwordx4 v114, s[40:43], s52 offen offset:2048 lds\n\t"
    "ds_read_b128 v[60:63], v101\n\t"
    "ds_read_b64_tr_b16 v[64:65], v104 offset:5120\n\t"
    "ds_read_b64_tr_b16 v[66:67], v104 offset:5376\n\t"
    "v_add_u32 v100, 28672, v100\n\t"
    "v_add_u32 v108, 28672, v108\n\t"
    "buffer_load_dwordx4 v115, s[40:43], s52 offen offset:3072 lds\n\t"
    "s_add_u32 m0, s56, 114688\n\t"
    "s_waitcnt lgkmcnt(3)\n\t"
    "v_mfma_f32_32x32x16_bf16 v[0:15], v[48:51], v[52:55], v[0:15]\n\t"
    "buffer_load_dwordx4 v116, s[44:47], s53 offen lds\n\t"
    "ds_read_b128 v[72:75], v102\n\t"
    "ds_read_b64_tr_b16 v[76:77], v104 offset:8192\n\t"
    "ds_read_b64_tr_b16 v[78:79], v104 offset:8448\n\t"
    "v_add_u32 v101, 28672, v101\n\t"
    "v_add_u32 v109, 28672, v109\n\t"
    "buffer_load_dwordx4 v116, s[44:47], s53 offen offset:1024 lds\n\t"
    "s_waitcnt lgkmcnt(3)\n\t"
    "v_mfma_f32_32x32x16_bf16 v[0:15], v[60:63], v[64:67], v[0:15]\n\t"
    "buffer_load_dwordx4 v116, s[44:47], s53 offen offset:2048 lds\n\t"
    "s_add_u32 s52, s52, 128\n\t"
    "s_and_b32 s52, s52, 0x3ff\n\t"
    "s_mul_i32 s53, s52, 1536\n\t"
    "ds_read_b128 v[84:87], v103\n\t"
    "ds_read_b64_tr_b16 v[88:89], v104 offset:11264\n\t"
    "ds_read_b64_tr_b16 v[90:91], v104 offset:11520\n\t"
    "v_add_u32 v102, 28672, v102\n\t"
    "v_add_u32 v110, 28672, v110\n\t"
    "v_add_u32 v103, 28672, v103\n\t"
    "v_add_u32 v111, 28672, v111\n\t"
    "v_add_u32 v104, 28672, v104\n\t"
    "s_cmp_eq_u32 s62, 1\n\t"
    "s_cbranch_scc1 41f\n\t"
    "31:\n\t"
    "s_waitcnt vmcnt(15)\n\t"
    "s_barrier\n\t"
    "s_add_u32 m0, s55, 0\n\t"
    "s_waitcnt lgkmcnt(3)\n\t"
    "v_mfma_f32_32x32x16_bf16 v[0:15], v[72:75], v[76:79], v[0:15]\n\t"
    "buffer_load_dwordx4 v112, s[40:43], s52 offen lds\n\t"
    "ds_read_b128 v[48:51], v100\n\t"
    "ds_read_b64_tr_b16 v[52:53], v104 offset:2048\n\t"
    "ds_read_b64_tr_b16 v[54:55], v104 offset:2304\n\t"
    "buffer_load_dwordx4 v113, s[40:43], s52 offen offset:1024 lds\n\t"
    "s_waitcnt lgkmcnt(3)\n\t"
    "v_mfma_f32_32x32x16_bf16 v[0:15], v[84:87], v[88:91], v[0:15]\n\t"
    "buffer_load_dwordx4 v114, s[40:43], s52 offen offset:2048 lds\n\t"
    "ds_read_b128 v[60:63], v101\n\t"
    "ds_read_b64_tr_b16 v[64:65], v104 offset:5120\n\t"
    "ds_read_b64_tr_b16 v[66:67], v104 offset:5376\n\t"
    "v_add_u32 v100, 28672, v100\n\t"
    "v_add_u32 v108, 28672, v108\n\t"
    "buffer_load_dwordx4 v115, s[40:43], s52 offen offset:3072 lds\n\t"
    "s_add_u32 m0, s56, 0\n\t"
    "s_waitcnt lgkmcnt(3)\n\t"
    "v_mfma_f32_32x32x16_bf16 v[0:15], v[48:51], v[52:55], v[0:15]\n\t"
    "buffer_load_dwordx4 v116, s[44:47], s53 offen lds\n\t"
    "ds_read_b128 v[72:75], v102\n\t"
    "ds_read_b64_tr_b16 v[76:77], v104 offset:8192\n\t"
    "ds_read_b64_tr_b16 v[78:79], v104 offset:8448\n\t"
    "v_add_u32 v101, 28672, v101\n\t"
    "v_add_u32 v109, 28672, v109\n\t"
    "buffer_load_dwordx4 v116, s[44:47], s53 offen offset:1024 lds\n\t"
    "s_waitcnt lgkmcnt(3)\n\t"
    "v_mfma_f32_32x32x16_bf16 v[0:15], v[60:63], v[64:67], v[0:15]\n\t"
    "buffer_load_dwordx4 v116, s[44:47], s53 offen offset:2048 lds\n\t"
    "s_add_u32 s52, s52, 128\n\t"
    "s_and_b32 s52, s52, 0x3ff\n\t"
    "s_mul_i32 s53, s52, 1536\n\t"
    "ds_read_b128 v[84:87], v103\n\t"
    "ds_read_b64_tr_b16 v[88:89], v104 offset:11264\n\t"
    "ds_read_b64_tr_b16 v[90:91], v104 offset:11520\n\t"
    "v_add_u32 v102, 28672, v102\n\t"
    "v_add_u32 v110, 28672, v110\n\t"
    "v_add_u32 v103, 28672, v103\n\t"
    "v_add_u32 v111, 28672, v111\n\t"
    "v_add_u32 v104, 28672, v104\n\t"
    "s_cmp_eq_u32 s62, 2\n\t"
    "s_cbranch_scc1 42f\n\t"
    "32:\n\t"
    "s_waitcnt vmcnt(14)\n\t"
    "s_barrier\n\t"
    "s_add_u32 m0, s55, 28672\n\t"
    "s_waitcnt lgkmcnt(3)\n\t"
    "v_mfma_f32_32x32x16_bf16 v[0:15], v[72:75], v[76:79], v[0:15]\n\t"
    "buffer_load_dwordx4 v112, s[40:43], s52 offen lds\n\t"
    "ds_read_b128 v[48:51], v100\n\t"
    "ds_read_b64_tr_b16 v[52:53], v104 offset:2048\n\t"
    "ds_read_b64_tr_b16 v[54:55], v104 offset:2304\n\t"
    "buffer_load_dwordx4 v113, s[40:43], s52 offen offset:1024 lds\n\t"
    "s_waitcnt lgkmcnt(3)\n\t"
    "v_mfma_f32_32x32x16_bf16 v[0:15], v[84:87], v[88:91], v[0:15]\n\t"
    "buffer_load_dwordx4 v114, s[40:43], s52 offen offset:2048 lds\n\t"
    "ds_read_b128 v[60:63], v101\n\t"
    "ds_read_b64_tr_b16 v[64:65], v104 offset:5120\n\t"
    "ds_read_b64_tr_b16 v[66:67], v104 offset:5376\n\t"
    "v_add_u32 v100, 28672, v100\n\t"
    "v_add_u32 v108, 28672, v108\n\t"
    "buffer_load_dwordx4 v115, s[40:43], s52 offen offset:3072 lds\n\t"
    "s_add_u32 m0, s56, 28672\n\t"
    "s_waitcnt lgkmcnt(3)\n\t"
    "v_mfma_f32_32x32x16_bf16 v[0:15], v[48:51], v[52:55], v[0:15]\n\t"
    "buffer_load_dwordx4 v116, s[44:47], s53 offen lds\n\t"
    "ds_read_b128 v[72:75], v102\n\t"
    "ds_read_b64_tr_b16 v[76:77], v104 offset:8192\n\t"
    "ds_read_b64_tr_b16 v[78:79], v104 offset:8448\n\t"
    "v_add_u32 v101, 28672, v101\n\t"
    "v_add_u32 v109, 28672, v109\n\t"
    "buffer_load_dwordx4 v116, s[44:47], s53 offen offset:1024 lds\n\t"
    "s_waitcnt lgkmcnt(3)\n\t"
    "v_mfma_f32_32x32x16_bf16 v[0:15], v[60:63], v[64:67], v[0:15]\n\t"
    "buffer_load_dwordx4 v116, s[44:47], s53 offen offset:2048 lds\n\t"
    "s_add_u32 s52, s52, 128\n\t"
    "s_and_b32 s52, s52, 0x3ff\n\t"
    "s_mul_i32 s53, s52, 1536\n\t"
    "ds_read_b128 v[84:87], v103\n\t"
    "ds_read_b64_tr_b16 v[88:89], v104 offset:11264\n\t"
    "ds_read_b64_tr_b16 v[90:91], v104 offset:11520\n\t"
    "v_add_u32 v102, 28672, v102\n\t"
    "v_add_u32 v110, 28672, v110\n\t"
    "v_add_u32 v103, 28672, v103\n\t"
    "v_add_u32 v111, 28672, v111\n\t"
    "v_add_u32 v104, 28672, v104\n\t"
    "s_cmp_eq_u32 s62, 3\n\t"
    "s_cbranch_scc1 43f\n\t"
    "33:\n\t"
    "s_waitcnt vmcnt(14)\n\t"
    "s_barrier\n\t"
    "s_add_u32 m0, s55, 57344\n\t"
    "s_waitcnt lgkmcnt(3)\n\t"
    "v_mfma_f32_32x32x16_bf16 v[0:15], v[72:75], v[76:79], v[0:15]\n\t"
    "buffer_load_dwordx4 v112, s[40:43], s52 offen lds\n\t"
    "ds_read_b128 v[48:51], v100\n\t"
    "ds_read_b64_tr_b16 v[52:53], v104 offset:2048\n\t"
    "ds_read_b64_tr_b16 v[54:55], v104 offset:2304\n\t"
    "buffer_load_dwordx4 v113, s[40:43], s52 offen offset:1024 lds\n\t"
    "s_waitcnt lgkmcnt(3)\n\t"
    "v_mfma_f32_32x32x16_bf16 v[0:15], v[84:87], v[88:91], v[0:15]\n\t"
    "buffer_load_dwordx4 v114, s[40:43], s52 offen offset:2048 lds\n\t"
    "ds_read_b128 v[60:63], v101\n\t"
    "ds_read_b64_tr_b16 v[64:65], v104 offset:5120\n\t"
    "ds_read_b64_tr_b16 v[66:67], v104 offset:5376\n\t"
    "v_add_u32 v100, 4294852608, v100\n\t"
    "v_add_u32 v108, 4294852608, v108\n\t"
    "buffer_load_dwordx4 v115, s[40:43], s52 offen offset:3072 lds\n\t"
    "s_add_u32 m0, s56, 57344\n\t"
    "s_waitcnt lgkmcnt(3)\n\t"
    "v_mfma_f32_32x32x16_bf16 v[0:15], v[48:51], v[52:55], v[0:15]\n\t"
    "buffer_load_dwordx4 v116, s[44:47], s53 offen lds\n\t"
    "ds_read_b128 v[72:75], v102\n\t"
    "ds_read_b64_tr_b16 v[76:77], v104 offset:8192\n\t"
    "ds_read_b64_tr_b16 v[78:79], v104 offset:8448\n\t"
    "v_add_u32 v101, 4294852608, v101\n\t"
    "v_add_u32 v109, 4294852608, v109\n\t"
    "buffer_load_dwordx4 v116, s[44:47], s53 offen offset:1024 lds\n\t"
    "s_waitcnt lgkmcnt(3)\n\t"
    "v_mfma_f32_32x32x16_bf16 v[0:15], v[60:63], v[64:67], v[0:15]\n\t"
    "buffer_load_dwordx4 v116, s[44:47], s53 offen offset:2048 lds\n\t"
    "s_add_u32 s52, s52, 512\n\t"
    "s_and_b32 s52, s52, 0x3ff\n\t"
    "s_mul_i32 s53, s52, 1536\n\t"
    "ds_read_b128 v[84:87], v103\n\t"
    "ds_read_b64_tr_b16 v[88:89], v104 offset:11264\n\t"
    "ds_read_b64_tr_b16 v[90:91], v104 offset:11520\n\t"
    "v_add_u32 v102, 4294852608, v102\n\t"
    "v_add_u32 v110, 4294852608, v110\n\t"
    "v_add_u32 v103, 4294852608, v103\n\t"
    "v_add_u32 v111, 4294852608, v111\n\t"
    "v_add_u32 v104, 4294852608, v104\n\t"
    "s_cmp_eq_u32 s62, 4\n\t"
    "s_cbranch_scc1 44f\n\t"
    "34:\n\t"
    "s_waitcnt vmcnt(14)\n\t"
    "s_barrier\n\t"
    "s_add_u32 m0, s56, 86016\n\t"
    "s_waitcnt lgkmcnt(3)\n\t"
    "v_mfma_f32_32x32x16_bf16 v[0:15], v[72:75], v[76:79], v[0:15]\n\t"
    "buffer_load_dwordx4 v120, s[48:51], s52 offen lds\n\t"
    "ds_read_b128 v[48:51], v100\n\t"
    "ds_read_b64_tr_b16 v[52:53], v104 offset:2048\n\t"
    "ds_read_b64_tr_b16 v[54:55], v104 offset:2304\n\t"
    "s_waitcnt lgkmcnt(3)\n\t"
    "v_mfma_f32_32x32x16_bf16 v[0:15], v[84:87], v[88:91], v[0:15]\n\t"
    "ds_read_b128 v[60:63], v101\n\t"
    "ds_read_b64_tr_b16 v[64:65], v104 offset:5120\n\t"
    "ds_read_b64_tr_b16 v[66:67], v104 offset:5376\n\t"
    "v_add_u32 v100, 28672, v100\n\t"
    "v_add_u32 v108, 28672, v108\n\t"
    "s_waitcnt lgkmcnt(3)\n\t"
    "v_mfma_f32_32x32x16_bf16 v[0:15], v[48:51], v[52:55], v[0:15]\n\t"
    "buffer_load_dwordx4 v121, s[48:51], s52 offen offset:1024 lds\n\t"
    "s_add_u32 s52, s52, 128\n\t"
    "s_and_b32 s52, s52, 0x3ff\n\t"
    "ds_read_b128 v[72:75], v102\n\t"
    "ds_read_b64_tr_b16 v[76:77], v104 offset:8192\n\t"
    "ds_read_b64_tr_b16 v[78:79], v104 offset:8448\n\t"
    "v_add_u32 v101, 28672, v101\n\t"
    "v_add_u32 v109, 28672, v109\n\t"
    "s_waitcnt lgkmcnt(3)\n\t"
    "v_mfma_f32_32x32x16_bf16 v[0:15], v[60:63], v[64:67], v[0:15]\n\t"
    "ds_read_b128 v[84:87], v103\n\t"
    "ds_read_b64_tr_b16 v[88:89], v104 offset:11264\n\t"
    "ds_read_b64_tr_b16 v[90:91], v104 offset:11520\n\t"
    "v_add_u32 v102, 28672, v102\n\t"
    "v_add_u32 v110, 28672, v110\n\t"
    "v_add_u32 v103, 28672, v103\n\t"
    "v_add_u32 v111, 28672, v111\n\t"
    "v_add_u32 v104, 28672, v104\n\t"
    "s_cmp_eq_u32 s62, 5\n\t"
    "s_cbranch_scc1 45f\n\t"
    "35:\n\t"
    "s_waitcnt vmcnt(9)\n\t"
    "s_barrier\n\t"
    "s_add_u32 m0, s56, 114688\n\t"
    "s_waitcnt lgkmcnt(3)\n\t"
    "v_mfma_f32_32x32x16_bf16 v[0:15], v[72:75], v[76:79], v[0:15]\n\t"
    "buffer_load_dwordx4 v120, s[48:51], s52 offen lds\n\t"
    "ds_read_b128 v[48:51], v100\n\t"
    "ds_read_b64_tr_b16 v[52:53], v104 offset:2048\n\t"
    "ds_read_b64_tr_b16 v[54:55], v104 offset:2304\n\t"
    "s_waitcnt lgkmcnt(3)\n\t"
    "v_mfma_f32_32x32x16_bf16 v[0:15], v[84:87], v[88:91], v[0:15]\n\t"
    "ds_read_b128 v[60:63], v101\n\t"
    "ds_read_b64_tr_b16 v[64:65], v104 offset:5120\n\t"
    "ds_read_b64_tr_b16 v[66:67], v104 offset:5376\n\t"
    "v_add_u32 v100, 28672, v100\n\t"
    "v_add_u32 v108, 28672, v108\n\t"
    "s_waitcnt lgkmcnt(3)\n\t"
    "v_mfma_f32_32x32x16_bf16 v[0:15], v[48:51], v[52:55], v[0:15]\n\t"
    "buffer_load_dwordx4 v121, s[48:51], s52 offen offset:1024 lds\n\t"
    "s_add_u32 s52, s52, 128\n\t"
    "s_and_b32 s52, s52, 0x3ff\n\t"
    "ds_read_b128 v[72:75], v102\n\t"
    "ds_read_b64_tr_b16 v[76:77], v104 offset:8192\n\t"
    "ds_read_b64_tr_b16 v[78:79], v104 offset:8448\n\t"
    "v_add_u32 v101, 28672, v101\n\t"
    "v_add_u32 v109, 28672, v109\n\t"
    "s_waitcnt lgkmcnt(3)\n\t"
    "v_mfma_f32_32x32x16_bf16 v[0:15], v[60:63], v[64:67], v[0:15]\n\t"
    "ds_read_b128 v[84:87], v103\n\t"
    "ds_read_b64_tr_b16 v[88:89], v104 offset:11264\n\t"
    "ds_read_b64_tr_b16 v[90:91], v104 offset:11520\n\t"
    "v_add_u32 v102, 28672, v102\n\t"
    "v_add_u32 v110, 28672, v110\n\t"
    "v_add_u32 v103, 28672, v103\n\t"
    "v_add_u32 v111, 28672, v111\n\t"
    "v_add_u32 v104, 28672, v104\n\t"
    "s_cmp_eq_u32 s62, 6\n\t"
    "s_cbranch_scc1 46f\n\t"
    "36:\n\t"
    "s_waitcnt vmcnt(4)\n\t"
    "s_barrier\n\t"
    "s_add_u32 m0, s56, 0\n\t"
    "s_waitcnt lgkmcnt(3)\n\t"
    "v_mfma_f32_32x32x16_bf16 v[0:15], v[72:75], v[76:79], v[0:15]\n\t"
    "buffer_load_dwordx4 v120, s[48:51], s52 offen lds\n\t"
    "ds_read_b128 v[48:51], v100\n\t"
    "ds_read_b64_tr_b16 v[52:53], v104 offset:2048\n\t"
    "ds_read_b64_tr_b16 v[54:55], v104 offset:2304\n\t"
    "s_waitcnt lgkmcnt(3)\n\t"
    "v_mfma_f32_32x32x16_bf16 v[0:15], v[84:87], v[88:91], v[0:15]\n\t"
    "ds_read_b128 v[60:63], v101\n\t"
    "ds_read_b64_tr_b16 v[64:65], v104 offset:5120\n\t"
    "ds_read_b64_tr_b16 v[66:67], v104 offset:5376\n\t"
    "v_add_u32 v100, 28672, v100\n\t"
    "v_add_u32 v108, 28672, v108\n\t"
    "s_waitcnt lgkmcnt(3)\n\t"
    "v_mfma_f32_32x32x16_bf16 v[0:15], v[48:51], v[52:55], v[0:15]\n\t"
    "buffer_load_dwordx4 v121, s[48:51], s52 offen offset:1024 lds\n\t"
    "s_add_u32 s52, s52, 128\n\t"
    "s_and_b32 s52, s52, 0x3ff\n\t"
    "ds_read_b128 v[72:75], v102\n\t"
    "ds_read_b64_tr_b16 v[76:77], v104 offset:8192\n\t"
    "ds_read_b64_tr_b16 v[78:79], v104 offset:8448\n\t"
    "v_add_u32 v101, 28672, v101\n\t"
    "v_add_u32 v109, 28672, v109\n\t"
    "s_waitcnt lgkmcnt(3)\n\t"
    "v_mfma_f32_32x32x16_bf16 v[0:15], v[60:63], v[64:67], v[0:15]\n\t"
    "ds_read_b128 v[84:87], v103\n\t"
    "ds_read_b64_tr_b16 v[88:89], v104 offset:11264\n\t"
    "ds_read_b64_tr_b16 v[90:91], v104 offset:11520\n\t"
    "v_add_u32 v102, 28672, v102\n\t"
    "v_add_u32 v110, 28672, v110\n\t"
    "v_add_u32 v103, 28672, v103\n\t"
    "v_add_u32 v111, 28672, v111\n\t"
    "v_add_u32 v104, 28672, v104\n\t"
    "s_cmp_eq_u32 s62, 7\n\t"
    "s_cbranch_scc1 47f\n\t"
    "37:\n\t"
    "s_waitcnt vmcnt(4)\n\t"
    "s_barrier\n\t"
    "s_add_u32 m0, s56, 28672\n\t"
    "s_waitcnt lgkmcnt(3)\n\t"
    "v_mfma_f32_32x32x16_bf16 v[0:15], v[72:75], v[76:79], v[0:15]\n\t"
    "buffer_load_dwordx4 v120, s[48:51], s52 offen lds\n\t"
    "ds_read_b128 v[48:51], v100\n\t"
    "ds_read_b128 v[52:55], v108\n\t"
    "s_waitcnt lgkmcnt(2)\n\t"
    "v_mfma_f32_32x32x16_bf16 v[0:15], v[84:87], v[88:91], v[0:15]\n\t"
    "ds_read_b128 v[60:63], v101\n\t"
    "ds_read_b128 v[64:67], v109\n\t"
    "v_add_u32 v100, 28672, v100\n\t"
    "v_add_u32 v108, 28672, v108\n\t"
    "s_waitcnt lgkmcnt(2)\n\t"
    "v_mfma_f32_32x32x16_bf16 v[32:47], v[48:51], v[52:55], v[32:47]\n\t"
    "buffer_load_dwordx4 v121, s[48:51], s52 offen offset:1024 lds\n\t"
    "s_add_u32 s52, s52, 128\n\t"
    "s_and_b32 s52, s52, 0x3ff\n\t"
    "ds_read_b128 v[72:75], v102\n\t"
    "ds_read_b128 v[76:79], v110\n\t"
    "v_add_u32 v101, 28672, v101\n\t"
    "v_add_u32 v109, 28672, v109\n\t"
    "s_waitcnt lgkmcnt(2)\n\t"
    "v_mfma_f32_32x32x16_bf16 v[32:47], v[60:63], v[64:67], v[32:47]\n\t"
    "ds_read_b128 v[84:87], v103\n\t"
    "ds_read_b128 v[88:91], v111\n\t"
    "v_add_u32 v102, 28672, v102\n\t"
    "v_add_u32 v110, 28672, v110\n\t"
    "v_add_u32 v103, 28672, v103\n\t"
    "v_add_u32 v111, 28672, v111\n\t"
    "s_waitcnt lgkmcnt(0)\n\t"
    "s_branch 7f\n\t"
    "2:\n\t"
    "ds_read_b128 v[136:139], v124 offset:0\n\t"
    "ds_read_b128 v[140:143], v124 offset:32\n\t"
    "ds_read_b128 v[144:147], v124 offset:64\n\t"
    "ds_read_b128 v[148:151], v124 offset:96\n\t"
    "v_mov_b32 v126, 0\n\t"
    "v_mov_b32 v127, 0\n\t"
    "s_waitcnt lgkmcnt(0)\n\t"
    "v_dot2c_f32_bf16 v126, v48, v136\n\t"
    "v_dot2c_f32_bf16 v127, v49, v137\n\t"
    "v_dot2c_f32_bf16 v126, v50, v138\n\t"
    "v_dot2c_f32_bf16 v127, v51, v139\n\t"
    "v_dot2c_f32_bf16 v126, v60, v140\n\t"
    "v_dot2c_f32_bf16 v127, v61, v141\n\t"
    "v_dot2c_f32_bf16 v126, v62, v142\n\t"
    "v_dot2c_f32_bf16 v127, v63, v143\n\t"
    "v_dot2c_f32_bf16 v126, v72, v144\n\t"
    "v_dot2c_f32_bf16 v127, v73, v145\n\t"
    "v_dot2c_f32_bf16 v126, v74, v146\n\t"
    "v_dot2c_f32_bf16 v127, v75, v147\n\t"
    "v_dot2c_f32_bf16 v126, v84, v148\n\t"
    "v_dot2c_f32_bf16 v127, v85, v149\n\t"
    "v_dot2c_f32_bf16 v126, v86, v150\n\t"
    "v_dot2c_f32_bf16 v127, v87, v151\n\t"
    "s_nop 3\n\t"
    "v_add_f32 v126, v126, v127\n\t"
    "s_setpc_b64 s[60:61]\n\t"
    "40:\n\t"
    "s_call_b64 s[60:61], 2b\n\t"
    "s_branch 30b\n\t"
    "41:\n\t"
    "s_call_b64 s[60:61], 2b\n\t"
    "s_branch 31b\n\t"
    "42:\n\t"
    "s_call_b64 s[60:61], 2b\n\t"
    "s_branch 32b\n\t"
    "43:\n\t"
    "s_call_b64 s[60:61], 2b\n\t"
    "s_branch 33b\n\t"
    "44:\n\t"
    "s_call_b64 s[60:61], 2b\n\t"
    "s_branch 34b\n\t"
    "45:\n\t"
    "s_call_b64 s[60:61], 2b\n\t"
    "s_branch 35b\n\t"
    "46:\n\t"
    "s_call_b64 s[60:61], 2b\n\t"
    "s_branch 36b\n\t"
    "47:\n\t"
    "s_call_b64 s[60:61], 2b\n\t"
    "s_branch 37b\n\t"
    "7:\n\t"
    "v_add_co_u32 v230, vcc, 1, v230\n\t"
    "s_nop 1\n\t"
    "v_addc_co_u32 v231, vcc, 0, v231, vcc\n\t"
        : "={v[0:15]}"(av), "={v[32:47]}"(ae), "+{s52}"(soff), "+{s53}"(s53), "+{v[100:103]}"(ax), "+{v104}"(aw), "+{v[108:111]}"(ad), "={v126}"(z),
          "={v[230:231]}"(tag), "={v[48:63]}"(fa), "={v[64:79]}"(fb), "={v[80:95]}"(fc)
        : "{s[40:43]}"(sx), "{s[44:47]}"(sw), "{s[48:51]}"(sd), "{s55}"(m0x), "{s56}"(m0w), "{s62}"((nt - P1ROT) & 7), "{s[72:73]}"(fw),
          "{v[112:115]}"(vx), "{v116}"(vw), "{v120}"(d0), "{v121}"(d1), "{v124}"(l0 + LDS_GW + (nt & 7) * 128 + h * 16)
        : "memory", "scc", "vcc", "s60", "s61", "v127", "v136", "v137", "v138", "v139", "v140", "v141", "v142", "v143", "v144", "v145", "v146", "v147", "v148", "v149", "v150", "v151");
    {
      const char* vd = ws + WS_KV + (size_t)(2 * nt * 64 + tt1) * 2048 + 1024;
      const unsigned vv = (r >> 4) * 131072 + h * 256 + (r & 15) * 16;
      st16s(vd, vv, pack8(av, 0, 1.f));
      st16s(vd, vv + 512, pack8(av, 8, 1.f));
    }
    u32x8 q;
    P2_STATE
    asm volatile(
    HZ
    "s_waitcnt vmcnt(6)\n\t"
    "s_barrier\n\t"
    "s_add_u32 m0, s56, 57344\n\t"
    "s_waitcnt lgkmcnt(2)\n\t"
    "v_mfma_f32_32x32x16_bf16 v[32:47], v[72:75], v[76:79], v[32:47]\n\t"
    "buffer_load_dwordx4 v120, s[48:51], s52 offen lds\n\t"
    "ds_read_b128 v[48:51], v100\n\t"
    "ds_read_b128 v[52:55], v108\n\t"
    "s_waitcnt lgkmcnt(2)\n\t"
    "v_mfma_f32_32x32x16_bf16 v[32:47], v[84:87], v[88:91], v[32:47]\n\t"
    "ds_read_b128 v[60:63], v101\n\t"
    "ds_read_b128 v[64:67], v109\n\t"
    "v_add_u32 v100, 4294852608, v100\n\t"
    "v_add_u32 v108, 4294852608, v108\n\t"
    "s_waitcnt lgkmcnt(2)\n\t"
    "v_mfma_f32_32x32x16_bf16 v[32:47], v[48:51], v[52:55], v[32:47]\n\t"
    "buffer_load_dwordx4 v121, s[48:51], s52 offen offset:1024 lds\n\t"
    "s_add_u32 s52, s52, 128\n\t"
    "s_and_b32 s52, s52, 0x3ff\n\t"
    "ds_read_b128 v[72:75], v102\n\t"
    "ds_read_b128 v[76:79], v110\n\t"
    "v_add_u32 v101, 4294852608, v101\n\t"
    "v_add_u32 v109, 4294852608, v109\n\t"
    "v_mov_b64 v[24:25], 0\n\t"
    "v_mov_b64 v[26:27], 0\n\t"
    "v_mov_b64 v[28:29], 0\n\t"
    "v_mov_b64 v[30:31], 0\n\t"
    "v_mov_b64 v[156:157], 0\n\t"
    "v_mov_b64 v[158:159], 0\n\t"
    "v_mov_b64 v[160:161], 0\n\t"
    "v_mov_b64 v[162:163], 0\n\t"
    "v_mov_b64 v[164:165], 0\n\t"
    "v_mov_b64 v[166:167], 0\n\t"
    "v_mov_b64 v[168:169], 0\n\t"
    "v_mov_b64 v[170:171], 0\n\t"
    "v_mov_b64 v[172:173], 0\n\t"
    "v_mov_b64 v[174:175], 0\n\t"
    "s_waitcnt lgkmcnt(2)\n\t"
    "v_mfma_f32_32x32x16_bf16 v[32:47], v[60:63], v[64:67], v[32:47]\n\t"
    "ds_read_b128 v[84:87], v103\n\t"
    "ds_read_b128 v[88:91], v111\n\t"
    "v_add_u32 v102, 4294852608, v102\n\t"
    "v_add_u32 v110, 4294852608, v110\n\t"
    "v_add_u32 v103, 4294852608, v103\n\t"
    "v_add_u32 v111, 4294852608, v111\n\t"
    "s_waitcnt vmcnt(6)\n\t"
    "s_barrier\n\t"
    "s_add_u32 m0, s55, 86016\n\t"
    "s_waitcnt lgkmcnt(2)\n\t"
    "v_mfma_f32_32x32x16_bf16 v[32:47], v[72:75], v[76:79], v[32:47]\n\t"
    "buffer_load_dwordx4 v112, s[40:43], s52 offen lds\n\t"
    "ds_read_b128 v[48:51], v100\n\t"
    "ds_read_b128 v[52:55], v108\n\t"
    "buffer_load_dwordx4 v113, s[40:43], s52 offen offset:1024 lds\n\t"
    "s_waitcnt lgkmcnt(2)\n\t"
    "v_mfma_f32_32x32x16_bf16 v[32:47], v[84:87], v[88:91], v[32:47]\n\t"
    "buffer_load_dwordx4 v114, s[40:43], s52 offen offset:2048 lds\n\t"
    "ds_read_b128 v[60:63], v101\n\t"
    "ds_read_b128 v[64:67], v109\n\t"
    "v_add_u32 v100, 28672, v100\n\t"
    "v_add_u32 v108, 28672, v108\n\t"
    "s_waitcnt lgkmcnt(2)\n\t"
    "v_mfma_f32_32x32x16_bf16 v[32:47], v[48:51], v[52:55], v[32:47]\n\t"
    "buffer_load_dwordx4 v115, s[40:43], s52 offen offset:3072 lds\n\t"
    "ds_read_b128 v[72:75], v102\n\t"
    "ds_read_b128 v[76:79], v110\n\t"
    "s_add_u32 m0, s56, 86016\n\t"
    "v_add_u32 v101, 28672, v101\n\t"
    "v_add_u32 v109, 28672, v109\n\t"
    "v_mov_b64 v[176:177], 0\n\t"
    "v_mov_b64 v[178:179], 0\n\t"
    "v_mov_b64 v[180:181], 0\n\t"
    "v_mov_b64 v[182:183], 0\n\t"
    "v_mov_b64 v[184:185], 0\n\t"
    "v_mov_b64 v[186:187], 0\n\t"
    "v_mov_b64 v[188:189], 0\n\t"
    "v_mov_b64 v[190:191], 0\n\t"
    "v_mov_b64 v[192:193], 0\n\t"
    "v_mov_b64 v[194:195], 0\n\t"
    "v_mov_b64 v[196:197], 0\n\t"
    "v_mov_b64 v[198:199], 0\n\t"
    "v_mov_b64 v[200:201], 0\n\t"
    "v_mov_b64 v[202:203], 0\n\t"
    "buffer_load_dwordx4 v120, s[48:51], s52 offen lds\n\t"
    "s_waitcnt lgkmcnt(2)\n\t"
    "v_mfma_f32_32x32x16_bf16 v[32:47], v[60:63], v[64:67], v[32:47]\n\t"
    "buffer_load_dwordx4 v121, s[48:51], s52 offen offset:1024 lds\n\t"
    "s_add_u32 s52, s52, 128\n\t"
    "s_and_b32 s52, s52, 0x3ff\n\t"
    "ds_read_b128 v[84:87], v103\n\t"
    "ds_read_b128 v[88:91], v111\n\t"
    "v_add_u32 v102, 28672, v102\n\t"
    "v_add_u32 v110, 28672, v110\n\t"
    "v_add_u32 v103, 28672, v103\n\t"
    "v_add_u32 v111, 28672, v111\n\t"
    "s_waitcnt vmcnt(8)\n\t"
    "s_barrier\n\t"
    "s_add_u32 m0, s55, 114688\n\t"
    "s_waitcnt lgkmcnt(2)\n\t"
    "v_mfma_f32_32x32x16_bf16 v[32:47], v[72:75], v[76:79], v[32:47]\n\t"
    "buffer_load_dwordx4 v112, s[40:43], s52 offen lds\n\t"
    "ds_read_b128 v[48:51], v100\n\t"
    "ds_read_b128 v[52:55], v108\n\t"
    "buffer_load_dwordx4 v113, s[40:43], s52 offen offset:1024 lds\n\t"
    "s_waitcnt lgkmcnt(2)\n\t"
    "v_mfma_f32_32x32x16_bf16 v[32:47], v[84:87], v[88:91], v[32:47]\n\t"
    "buffer_load_dwordx4 v114, s[40:43], s52 offen offset:2048 lds\n\t"
    "ds_read_b128 v[60:63], v101\n\t"
    "ds_read_b128 v[64:67], v109\n\t"
    "v_add_u32 v100, 28672, v100\n\t"
    "v_add_u32 v108, 28672, v108\n\t"
    "s_waitcnt lgkmcnt(2)\n\t"
    "v_mfma_f32_32x32x16_bf16 v[32:47], v[48:51], v[52:55], v[32:47]\n\t"
    "buffer_load_dwordx4 v115, s[40:43], s52 offen offset:3072 lds\n\t"
    "ds_read_b128 v[72:75], v102\n\t"
    "ds_read_b128 v[76:79], v110\n\t"
    "s_add_u32 m0, s56, 114688\n\t"
    "v_add_u32 v101, 28672, v101\n\t"
    "v_add_u32 v109, 28672, v109\n\t"
    "v_mov_b64 v[238:239], 0\n\t"
    "v_mov_b64 v[240:241], 0\n\t"
    "v_mov_b64 v[242:243], 0\n\t"
    "v_mov_b64 v[244:245], 0\n\t"
    "v_mov_b64 v[246:247], 0\n\t"
    "v_mov_b64 v[248:249], 0\n\t"
    "v_mov_b64 v[250:251], 0\n\t"
    "v_mov_b64 v[252:253], 0\n\t"
    "v_mov_b32 v140, v221\n\t"
    "v_mov_b32 v141, v221\n\t"
    "v_mov_b32 v142, v221\n\t"
    "v_mov_b32 v143, v221\n\t"
    "v_mov_b32 v144, v221\n\t"
    "v_mov_b32 v145, v221\n\t"
    "buffer_load_dwordx4 v120, s[48:51], s52 offen lds\n\t"
    "s_waitcnt lgkmcnt(2)\n\t"
    "v_mfma_f32_32x32x16_bf16 v[32:47], v[60:63], v[64:67], v[32:47]\n\t"
    "buffer_load_dwordx4 v121, s[48:51], s52 offen offset:1024 lds\n\t"
    "s_add_u32 s52, s52, 128\n\t"
    "s_and_b32 s52, s52, 0x3ff\n\t"
    "ds_read_b128 v[84:87], v103\n\t"
    "ds_read_b128 v[88:91], v111\n\t"
    "v_add_u32 v102, 28672, v102\n\t"
    "v_add_u32 v110, 28672, v110\n\t"
    "v_add_u32 v103, 28672, v103\n\t"
    "v_add_u32 v111, 28672, v111\n\t"
    "s_waitcnt vmcnt(12)\n\t"
    "s_barrier\n\t"
    "s_add_u32 m0, s55, 0\n\t"
    "s_waitcnt lgkmcnt(2)\n\t"
    "v_mfma_f32_32x32x16_bf16 v[32:47], v[72:75], v[76:79], v[32:47]\n\t"
    "buffer_load_dwordx4 v112, s[40:43], s52 offen lds\n\t"
    "ds_read_b128 v[48:51], v100\n\t"
    "ds_read_b128 v[52:55], v108\n\t"
    "buffer_load_dwordx4 v113, s[40:43], s52 offen offset:1024 lds\n\t"
    "s_waitcnt lgkmcnt(2)\n\t"
    "v_mfma_f32_32x32x16_bf16 v[32:47], v[84:87], v[88:91], v[32:47]\n\t"
    "buffer_load_dwordx4 v114, s[40:43], s52 offen offset:2048 lds\n\t"
    "ds_read_b128 v[60:63], v101\n\t"
    "ds_read_b128 v[64:67], v109\n\t"
    "v_add_u32 v100, 28672, v100\n\t"
    "v_add_u32 v108, 28672, v108\n\t"
    "s_waitcnt lgkmcnt(2)\n\t"
    "v_mfma_f32_32x32x16_bf16 v[32:47], v[48:51], v[52:55], v[32:47]\n\t"
    "buffer_load_dwordx4 v115, s[40:43], s52 offen offset:3072 lds\n\t"
    "ds_read_b128 v[72:75], v102\n\t"
    "ds_read_b128 v[76:79], v110\n\t"
    "s_add_u32 m0, s56, 0\n\t"
    "v_add_u32 v101, 28672, v101\n\t"
    "v_add_u32 v109, 28672, v109\n\t"
    "v_mov_b32 v146, v221\n\t"
    "v_mov_b32 v147, v221\n\t"
    "v_mov_b32 v148, v221\n\t"
    "v_mov_b32 v149, v221\n\t"
    "v_mov_b32 v150, v221\n\t"
    "v_mov_b32 v151, v221\n\t"
    "v_mov_b32 v152, v221\n\t"
    "v_mov_b32 v153, v221\n\t"
    "v_mov_b32 v154, v221\n\t"
    "v_mov_b32 v155, v221\n\t"
    "v_mov_b32 v219, 0xdddd\n\t"
    "buffer_load_dwordx4 v120, s[48:51], s52 offen lds\n\t"
    "s_waitcnt lgkmcnt(2)\n\t"
    "v_mfma_f32_32x32x16_bf16 v[32:47], v[60:63], v[64:67], v[32:47]\n\t"
    "buffer_load_dwordx4 v121, s[48:51], s52 offen offset:1024 lds\n\t"
    "s_add_u32 s52, s52, 128\n\t"
    "s_and_b32 s52, s52, 0x3ff\n\t"
    "ds_read_b128 v[84:87], v103\n\t"
    "ds_read_b128 v[88:91], v111\n\t"
    "v_add_u32 v102, 28672, v102\n\t"
    "v_add_u32 v110, 28672, v110\n\t"
    "v_add_u32 v103, 28672, v103\n\t"
    "v_add_u32 v111, 28672, v111\n\t"
    "s_waitcnt vmcnt(12)\n\t"
    "s_barrier\n\t"
    "s_waitcnt lgkmcnt(2)\n\t"
    "v_mfma_f32_32x32x16_bf16 v[32:47], v[72:75], v[76:79], v[32:47]\n\t"
    "ds_read_b128 v[48:51], v100\n\t"
    "ds_read_b128 v[52:55], v108\n\t"
    "s_waitcnt lgkmcnt(2)\n\t"
    "v_mfma_f32_32x32x16_bf16 v[32:47], v[84:87], v[88:91], v[32:47]\n\t"
    "ds_read_b128 v[60:63], v101\n\t"
    "ds_read_b128 v[64:67], v109\n\t"
    "v_add_u32 v100, 28672, v100\n\t"
    "v_add_u32 v108, 28672, v108\n\t"
    "s_waitcnt lgkmcnt(2)\n\t"
    "v_mfma_f32_32x32x16_bf16 v[32:47], v[48:51], v[52:55], v[32:47]\n\t"
    "ds_read_b128 v[72:75], v102\n\t"
    "ds_read_b128 v[76:79], v110\n\t"
    "v_add_u32 v101, 28672, v101\n\t"
    "v_add_u32 v109, 28672, v109\n\t"
    "s_waitcnt lgkmcnt(2)\n\t"
    "v_mfma_f32_32x32x16_bf16 v[32:47], v[60:63], v[64:67], v[32:47]\n\t"
    "ds_read_b128 v[84:87], v103\n\t"
    "ds_read_b128 v[88:91], v111\n\t"
    "v_add_u32 v102, 28672, v102\n\t"
    "v_add_u32 v110, 28672, v110\n\t"
    "v_add_u32 v103, 28672, v103\n\t"
    "v_add_u32 v111, 28672, v111\n\t"
    "s_waitcnt vmcnt(6)\n\t"
    "s_barrier\n\t"
    "global_load_dwordx4 v[128:131], v125, s[78:79] sc1\n\t"
    "global_load_dwordx4 v[132:135], v125, s[78:79] offset:1024 sc1\n\t"
    "s_waitcnt lgkmcnt(2)\n\t"
    "v_mfma_f32_32x32x16_bf16 v[32:47], v[72:75], v[76:79], v[32:47]\n\t"
    "ds_read_b128 v[48:51], v100\n\t"
    "ds_read_b128 v[52:55], v108\n\t"
    "s_waitcnt lgkmcnt(2)\n\t"
    "v_mfma_f32_32x32x16_bf16 v[32:47], v[84:87], v[88:91], v[32:47]\n\t"
    "ds_read_b128 v[60:63], v101\n\t"
    "ds_read_b128 v[64:67], v109\n\t"
    "v_add_u32 v100, 4294852608, v100\n\t"
    "v_add_u32 v108, 4294852608, v108\n\t"
    "s_waitcnt lgkmcnt(2)\n\t"
    "v_mfma_f32_32x32x16_bf16 v[32:47], v[48:51], v[52:55], v[32:47]\n\t"
    "ds_read_b128 v[72:75], v102\n\t"
    "ds_read_b128 v[76:79], v110\n\t"
    "v_add_u32 v101, 4294852608, v101\n\t"
    "v_add_u32 v109, 4294852608, v109\n\t"
    "s_waitcnt lgkmcnt(2)\n\t"
    "v_mfma_f32_32x32x16_bf16 v[32:47], v[60:63], v[64:67], v[32:47]\n\t"
    "ds_read_b128 v[84:87], v103\n\t"
    "ds_read_b128 v[88:91], v111\n\t"
    "v_add_u32 v102, 4294852608, v102\n\t"
    "v_add_u32 v110, 4294852608, v110\n\t"
    "v_add_u32 v103, 4294852608, v103\n\t"
    "v_add_u32 v111, 4294852608, v111\n\t"
    "s_waitcnt vmcnt(2)\n\t"
    "s_barrier\n\t"
    "s_waitcnt lgkmcnt(2)\n\t"
    "v_mfma_f32_32x32x16_bf16 v[32:47], v[72:75], v[76:79], v[32:47]\n\t"
    "ds_read_b128 v[48:51], v100\n\t"
    "ds_read_b128 v[52:55], v108\n\t"
    "s_waitcnt lgkmcnt(2)\n\t"
    "v_mfma_f32_32x32x16_bf16 v[32:47], v[84:87], v[88:91], v[32:47]\n\t"
    "ds_read_b128 v[60:63], v101\n\t"
    "ds_read_b128 v[64:67], v109\n\t"
    "v_add_u32 v100, 28672, v100\n\t"
    "v_add_u32 v108, 28672, v108\n\t"
    "s_waitcnt lgkmcnt(2)\n\t"
    "v_mfma_f32_32x32x16_bf16 v[32:47], v[48:51], v[52:55], v[32:47]\n\t"
    "ds_read_b128 v[72:75], v102\n\t"
    "ds_read_b128 v[76:79], v110\n\t"
    "v_add_u32 v101, 28672, v101\n\t"
    "v_add_u32 v109, 28672, v109\n\t"
    "s_waitcnt lgkmcnt(2)\n\t"
    "v_mfma_f32_32x32x16_bf16 v[32:47], v[60:63], v[64:67], v[32:47]\n\t"
    "ds_read_b128 v[84:87], v103\n\t"
    "ds_read_b128 v[88:91], v111\n\t"
    "v_add_u32 v102, 28672, v102\n\t"
    "v_add_u32 v110, 28672, v110\n\t"
    "v_add_u32 v103, 28672, v103\n\t"
    "v_add_u32 v111, 28672, v111\n\t"
    "s_waitcnt lgkmcnt(2)\n\t"
    "v_mfma_f32_32x32x16_bf16 v[32:47], v[72:75], v[76:79], v[32:47]\n\t"
    "s_waitcnt lgkmcnt(0)\n\t"
    "v_mfma_f32_32x32x16_bf16 v[32:47], v[84:87], v[88:91], v[32:47]\n\t"
    "s_barrier\n\t"
        : "+{v[32:47]}"(ae), "+{s52}"(soff), "+{v[100:103]}"(ax), "+{v[108:111]}"(ad), "={v[128:135]}"(q), "+{v[48:63]}"(fa), "+{v[64:79]}"(fb),
          "+{v[80:95]}"(fc), P2_INIT("=")
        : "{v221}"(t1), "{s[40:43]}"(sx), "{s[48:51]}"(sd), "{s55}"(m0x), "{s56}"(m0w), "{s[78:79]}"(qp), "{v[112:115]}"(vx), "{v120}"(d0), "{v121}"(d1),
          "{v125}"(lane * 16)
        : "memory", "scc");
    ST(1);
    asm volatile(
    "s_mov_b32 s60, 0xffff\n\t"
    "s_mov_b32 s61, 0xffff\n\t"
    "ds_read_b128 v[48:51], v220 offset:0\n\t"
    "ds_read_b128 v[136:139], v204 offset:16384\n\t"
    "s_mov_b64 exec, s[60:61]\n\t"
    "ds_read_b128 v[140:143], v205 offset:12288\n\t"
    "ds_read_b128 v[144:147], v205 offset:12800\n\t"
    "s_mov_b64 exec, -1\n\t"
    "s_nop 3\n\t"
        : P2_FIRST : "{v220}"(t0), P2_READS : "memory");
    const char* ab = ws + WS_AB + ((size_t)((nt >> 3) * 16 + 2 * (nt & 7) + 1) * 128 + 2 * tt1) * 1024;
    st16s(ab, lane * 16, swap_halves(pack8(ae, 0, 1.f)));
    st16s(ab, lane * 16 + 1024, swap_halves(pack8(ae, 8, 1.f)));
    {
      unsigned z2 = z;
      asm volatile("s_nop 1\n\tv_permlane32_swap_b32 %0, %1\n\ts_nop 1\n\tv_add_f32 %0, %0, %1\n\ts_mov_b64 exec, 0xffffffff\n\t"
                   "global_store_dword %2, %0, %3 sc1\n\ts_mov_b64 exec, -1"
                   : "+v"(z), "+v"(z2) : "v"(r * 32 + (nt & 7) * 4), "s"(ws + WS_G + (nt >> 3) * 65536 + tt1 * 1024) : "memory");
    }
    u32x4 ga;
    u32x8 gb;
    u32x16 gc, gd;
    unsigned unsafe;
    asm volatile(
    HZ
    "s_mov_b32 m0, s58\n\t"
    "s_waitcnt vmcnt(3) lgkmcnt(3)\n\t"
    "v_mfma_f32_32x32x16_bf16 v[0:15], v[48:51], v[128:131], 0\n\t"
    "v_mfma_f32_32x32x16_bf16 v[112:127], v[48:51], v[132:135], 0\n\t"
    "s_nop 11\n\t"
    "v_max3_f32 v220, v0, v1, v2\n\t"
    "v_max3_f32 v221, v3, v4, v5\n\t"
    "v_max3_f32 v220, v220, v6, v7\n\t"
    "v_max3_f32 v221, v221, v8, v9\n\t"
    "v_max3_f32 v220, v220, v10, v11\n\t"
    "v_max3_f32 v221, v221, v12, v13\n\t"
    "v_max3_f32 v220, v220, v14, v15\n\t"
    "v_max_f32 v220, v220, v221\n\t"
    "v_mov_b32 v221, v220\n\t"
    "s_nop 1\n\t"
    "v_permlane32_swap_b32 v220, v221\n\t"
    "s_nop 1\n\t"
    "v_max_f32 v220, v220, v221\n\t"
    "v_sub_f32 v221, 0, v220\n\t"
    "v_sub_f32 v220, 0, v220\n\t"
    "v_mov_b64 v[96:97], v[220:221]\n\t"
    "v_mov_b64 v[98:99], v[220:221]\n\t"
    "v_mov_b64 v[100:101], v[220:221]\n\t"
    "v_mov_b64 v[102:103], v[220:221]\n\t"
    "v_mov_b64 v[104:105], v[220:221]\n\t"
    "v_mov_b64 v[106:107], v[220:221]\n\t"
    "v_mov_b64 v[108:109], v[220:221]\n\t"
    "v_mov_b64 v[110:111], v[220:221]\n\t"
    "s_waitcnt lgkmcnt(2)\n\t"
    "s_nop 0\n\t"
    "v_mfma_f32_32x32x16_bf16 v[0:15], v[136:139], v[128:131], v[96:111]\n\t"
    "v_max3_f32 v220, v112, v113, v114\n\t"
    "v_max3_f32 v221, v115, v116, v117\n\t"
    "v_max3_f32 v220, v220, v118, v119\n\t"
    "v_max3_f32 v221, v221, v120, v121\n\t"
    "v_max3_f32 v220, v220, v122, v123\n\t"
    "v_max3_f32 v221, v221, v124, v125\n\t"
    "v_max3_f32 v220, v220, v126, v127\n\t"
    "v_max_f32 v220, v220, v221\n\t"
    "v_mov_b32 v221, v220\n\t"
    "s_nop 1\n\t"
    "v_permlane32_swap_b32 v220, v221\n\t"
    "s_nop 1\n\t"
    "v_max_f32 v220, v220, v221\n\t"
    "v_sub_f32 v221, 0, v220\n\t"
    "v_sub_f32 v220, 0, v220\n\t"
    "v_mov_b64 v[112:113], v[220:221]\n\t"
    "v_mov_b64 v[114:115], v[220:221]\n\t"
    "v_mov_b64 v[116:117], v[220:221]\n\t"
    "v_mov_b64 v[118:119], v[220:221]\n\t"
    "v_mov_b64 v[120:121], v[220:221]\n\t"
    "v_mov_b64 v[122:123], v[220:221]\n\t"
    "v_mov_b64 v[124:125], v[220:221]\n\t"
    "v_mov_b64 v[126:127], v[220:221]\n\t"
    "s_waitcnt lgkmcnt(0)\n\t"
    "s_barrier\n\t"
    "v_exp_f32 v0, v0\n\t"
    "v_exp_f32 v1, v1\n\t"
    "v_smfmac_f32_32x32x32_bf16 v[172:187], v[152:155], v[24:31], v219\n\t"
    "v_exp_f32 v2, v2\n\t"
    "v_exp_f32 v3, v3\n\t"
    "v_exp_f32 v4, v4\n\t"
    "v_mfma_f32_32x32x16_bf16 v[16:31], v[136:139], v[132:135], v[112:127]\n\t"
    "v_exp_f32 v5, v5\n\t"
    "s_mov_b64 exec, s[60:61]\n\t"
    "ds_read_b128 v[148:151], v205 offset:24576\n\t"
    "ds_read_b128 v[152:155], v205 offset:25088\n\t"
    "s_mov_b64 exec, -1\n\t"
    "v_exp_f32 v6, v6\n\t"
    "ds_read_b128 v[136:139], v204 offset:28672\n\t"
    "v_exp_f32 v7, v7\n\t"
    "v_exp_f32 v8, v8\n\t"
    "v_exp_f32 v9, v9\n\t"
    "v_exp_f32 v10, v10\n\t"
    "v_smfmac_f32_32x32x32_bf16 v[156:171], v[140:143], v[0:7], v219\n\t"
    "v_exp_f32 v11, v11\n\t"
    "v_exp_f32 v12, v12\n\t"
    "v_exp_f32 v13, v13\n\t"
    "v_exp_f32 v14, v14\n\t"
    "v_exp_f32 v15, v15\n\t"
    "v_exp_f32 v16, v16\n\t"
    "v_exp_f32 v17, v17\n\t"
    "v_smfmac_f32_32x32x32_bf16 v[156:171], v[144:147], v[8:15], v219\n\t"
    "v_exp_f32 v18, v18\n\t"
    "v_exp_f32 v19, v19\n\t"
    "s_waitcnt lgkmcnt(0)\n\t"
    "v_exp_f32 v20, v20\n\t"
    "v_mfma_f32_32x32x16_bf16 v[0:15], v[136:139], v[128:131], v[96:111]\n\t"
    "v_exp_f32 v21, v21\n\t"
    "v_exp_f32 v22, v22\n\t"
    "v_exp_f32 v23, v23\n\t"
    "v_exp_f32 v24, v24\n\t"
    "v_exp_f32 v25, v25\n\t"
    "v_exp_f32 v26, v26\n\t"
    "v_smfmac_f32_32x32x32_bf16 v[172:187], v[140:143], v[16:23], v219\n\t"
    "v_exp_f32 v27, v27\n\t"
    "v_exp_f32 v28, v28\n\t"
    "v_exp_f32 v29, v29\n\t"
    "v_exp_f32 v30, v30\n\t"
    "v_exp_f32 v31, v31\n\t"
    "s_waitcnt lgkmcnt(0)\n\t"
    "s_barrier\n\t"
    "v_exp_f32 v0, v0\n\t"
    "v_exp_f32 v1, v1\n\t"
    "v_smfmac_f32_32x32x32_bf16 v[172:187], v[144:147], v[24:31], v219\n\t"
    "v_exp_f32 v2, v2\n\t"
    "v_exp_f32 v3, v3\n\t"
    "v_exp_f32 v4, v4\n\t"
    "v_mfma_f32_32x32x16_bf16 v[16:31], v[136:139], v[132:135], v[112:127]\n\t"
    "v_exp_f32 v5, v5\n\t"
    "s_mov_b64 exec, s[60:61]\n\t"
    "ds_read_b128 v[140:143], v205 offset:36864\n\t"
    "ds_read_b128 v[144:147], v205 offset:37376\n\t"
    "s_mov_b64 exec, -1\n\t"
    "v_exp_f32 v6, v6\n\t"
    "ds_read_b128 v[136:139], v204 offset:40960\n\t"
    "v_exp_f32 v7, v7\n\t"
    "v_exp_f32 v8, v8\n\t"
    "v_exp_f32 v9, v9\n\t"
    "v_exp_f32 v10, v10\n\t"
    "v_smfmac_f32_32x32x32_bf16 v[156:171], v[148:151], v[0:7], v219\n\t"
    "v_exp_f32 v11, v11\n\t"
    "v_exp_f32 v12, v12\n\t"
    "v_exp_f32 v13, v13\n\t"
    "v_exp_f32 v14, v14\n\t"
    "v_exp_f32 v15, v15\n\t"
    "v_exp_f32 v16, v16\n\t"
    "v_exp_f32 v17, v17\n\t"
    "v_smfmac_f32_32x32x32_bf16 v[156:171], v[152:155], v[8:15], v219\n\t"
    "v_exp_f32 v18, v18\n\t"
    "v_exp_f32 v19, v19\n\t"
    "s_waitcnt lgkmcnt(0)\n\t"
    "v_exp_f32 v20, v20\n\t"
    "v_mfma_f32_32x32x16_bf16 v[0:15], v[136:139], v[128:131], v[96:111]\n\t"
    "v_exp_f32 v21, v21\n\t"
    "v_exp_f32 v22, v22\n\t"
    "v_exp_f32 v23, v23\n\t"
    "v_exp_f32 v24, v24\n\t"
    "v_exp_f32 v25, v25\n\t"
    "v_exp_f32 v26, v26\n\t"
    "v_smfmac_f32_32x32x32_bf16 v[172:187], v[148:151], v[16:23], v219\n\t"
    "v_exp_f32 v27, v27\n\t"
    "v_exp_f32 v28, v28\n\t"
    "v_exp_f32 v29, v29\n\t"
    "v_exp_f32 v30, v30\n\t"
    "v_exp_f32 v31, v31\n\t"
    "s_waitcnt vmcnt(0) lgkmcnt(0)\n\t"
    "s_barrier\n\t"
    "s_cmp_eq_u32 s55, 2\n\t"
    "s_cbranch_scc1 20f\n\t"
    "21:\n\t"
    "v_exp_f32 v0, v0\n\t"
    "v_exp_f32 v1, v1\n\t"
    "v_smfmac_f32_32x32x32_bf16 v[172:187], v[152:155], v[24:31], v219\n\t"
    "v_exp_f32 v2, v2\n\t"
    "global_load_lds_dword v218, s[76:77]\n\t"
    "v_exp_f32 v3, v3\n\t"
    "v_exp_f32 v4, v4\n\t"
    "v_mfma_f32_32x32x16_bf16 v[16:31], v[136:139], v[132:135], v[112:127]\n\t"
    "v_exp_f32 v5, v5\n\t"
    "s_mov_b64 exec, s[60:61]\n\t"
    "ds_read_b128 v[148:151], v205 offset:0\n\t"
    "ds_read_b128 v[152:155], v205 offset:512\n\t"
    "s_mov_b64 exec, -1\n\t"
    "v_exp_f32 v6, v6\n\t"
    "ds_read_b128 v[136:139], v204 offset:4096\n\t"
    "v_exp_f32 v7, v7\n\t"
    "v_exp_f32 v8, v8\n\t"
    "v_exp_f32 v9, v9\n\t"
    "v_exp_f32 v10, v10\n\t"
    "v_smfmac_f32_32x32x32_bf16 v[156:171], v[140:143], v[0:7], v219\n\t"
    "v_exp_f32 v11, v11\n\t"
    "v_exp_f32 v12, v12\n\t"
    "v_exp_f32 v13, v13\n\t"
    "v_exp_f32 v14, v14\n\t"
    "v_exp_f32 v15, v15\n\t"
    "v_exp_f32 v16, v16\n\t"
    "v_exp_f32 v17, v17\n\t"
    "v_smfmac_f32_32x32x32_bf16 v[156:171], v[144:147], v[8:15], v219\n\t"
    "v_exp_f32 v18, v18\n\t"
    "v_exp_f32 v19, v19\n\t"
    "s_waitcnt lgkmcnt(0)\n\t"
    "v_exp_f32 v20, v20\n\t"
    "v_mfma_f32_32x32x16_bf16 v[0:15], v[136:139], v[128:131], v[96:111]\n\t"
    "v_exp_f32 v21, v21\n\t"
    "v_exp_f32 v22, v22\n\t"
    "v_exp_f32 v23, v23\n\t"
    "v_exp_f32 v24, v24\n\t"
    "v_exp_f32 v25, v25\n\t"
    "v_exp_f32 v26, v26\n\t"
    "v_smfmac_f32_32x32x32_bf16 v[172:187], v[140:143], v[16:23], v219\n\t"
    "v_exp_f32 v27, v27\n\t"
    "v_exp_f32 v28, v28\n\t"
    "v_exp_f32 v29, v29\n\t"
    "v_exp_f32 v30, v30\n\t"
    "v_exp_f32 v31, v31\n\t"
    "s_waitcnt lgkmcnt(0)\n\t"
    "s_barrier\n\t"
    "v_exp_f32 v0, v0\n\t"
    "v_exp_f32 v1, v1\n\t"
    "v_smfmac_f32_32x32x32_bf16 v[172:187], v[144:147], v[24:31], v219\n\t"
    "v_exp_f32 v2, v2\n\t"
    "v_exp_f32 v3, v3\n\t"
    "v_exp_f32 v4, v4\n\t"
    "v_mfma_f32_32x32x16_bf16 v[16:31], v[136:139], v[132:135], v[112:127]\n\t"
    "v_exp_f32 v5, v5\n\t"
    "s_mov_b64 exec, s[60:61]\n\t"
    "ds_read_b128 v[140:143], v205 offset:12288\n\t"
    "ds_read_b128 v[144:147], v205 offset:12800\n\t"
    "s_mov_b64 exec, -1\n\t"
    "v_exp_f32 v6, v6\n\t"
    "ds_read_b128 v[136:139], v204 offset:16384\n\t"
    "v_exp_f32 v7, v7\n\t"
    "v_exp_f32 v8, v8\n\t"
    "v_exp_f32 v9, v9\n\t"
    "v_exp_f32 v10, v10\n\t"
    "v_smfmac_f32_32x32x32_bf16 v[156:171], v[148:151], v[0:7], v219\n\t"
    "v_exp_f32 v11, v11\n\t"
    "v_exp_f32 v12, v12\n\t"
    "v_exp_f32 v13, v13\n\t"
    "v_exp_f32 v14, v14\n\t"
    "v_exp_f32 v15, v15\n\t"
    "v_exp_f32 v16, v16\n\t"
    "v_exp_f32 v17, v17\n\t"
    "v_smfmac_f32_32x32x32_bf16 v[156:171], v[152:155], v[8:15], v219\n\t"
    "v_exp_f32 v18, v18\n\t"
    "v_exp_f32 v19, v19\n\t"
    "s_waitcnt lgkmcnt(0)\n\t"
    "v_exp_f32 v20, v20\n\t"
    "v_mfma_f32_32x32x16_bf16 v[0:15], v[136:139], v[128:131], v[96:111]\n\t"
    "v_exp_f32 v21, v21\n\t"
    "v_exp_f32 v22, v22\n\t"
    "v_exp_f32 v23, v23\n\t"
    "v_exp_f32 v24, v24\n\t"
    "v_exp_f32 v25, v25\n\t"
    "v_exp_f32 v26, v26\n\t"
    "v_smfmac_f32_32x32x32_bf16 v[172:187], v[148:151], v[16:23], v219\n\t"
    "v_exp_f32 v27, v27\n\t"
    "v_exp_f32 v28, v28\n\t"
    "v_exp_f32 v29, v29\n\t"
    "v_exp_f32 v30, v30\n\t"
    "v_exp_f32 v31, v31\n\t"
    "s_waitcnt lgkmcnt(0)\n\t"
    "s_barrier\n\t"
    "v_exp_f32 v0, v0\n\t"
    "v_exp_f32 v1, v1\n\t"
    "v_smfmac_f32_32x32x32_bf16 v[172:187], v[152:155], v[24:31], v219\n\t"
    "v_exp_f32 v2, v2\n\t"
    "v_exp_f32 v3, v3\n\t"
    "v_exp_f32 v4, v4\n\t"
    "v_mfma_f32_32x32x16_bf16 v[16:31], v[136:139], v[132:135], v[112:127]\n\t"
    "v_exp_f32 v5, v5\n\t"
    "s_mov_b64 exec, s[60:61]\n\t"
    "ds_read_b128 v[148:151], v205 offset:24576\n\t"
    "ds_read_b128 v[152:155], v205 offset:25088\n\t"
    "s_mov_b64 exec, -1\n\t"
    "v_exp_f32 v6, v6\n\t"
    "ds_read_b128 v[136:139], v204 offset:28672\n\t"
    "v_exp_f32 v7, v7\n\t"
    "v_exp_f32 v8, v8\n\t"
    "global_load_dwordx2 v[228:229], v226, s[62:63] sc1\n\t"
    "v_exp_f32 v9, v9\n\t"
    "v_exp_f32 v10, v10\n\t"
    "v_smfmac_f32_32x32x32_bf16 v[156:171], v[140:143], v[0:7], v219\n\t"
    "v_exp_f32 v11, v11\n\t"
    "v_exp_f32 v12, v12\n\t"
    "v_exp_f32 v13, v13\n\t"
    "v_exp_f32 v14, v14\n\t"
    "v_exp_f32 v15, v15\n\t"
    "v_exp_f32 v16, v16\n\t"
    "v_exp_f32 v17, v17\n\t"
    "v_smfmac_f32_32x32x32_bf16 v[156:171], v[144:147], v[8:15], v219\n\t"
    "v_exp_f32 v18, v18\n\t"
    "v_exp_f32 v19, v19\n\t"
    "s_waitcnt lgkmcnt(0)\n\t"
    "v_exp_f32 v20, v20\n\t"
    "v_mfma_f32_32x32x16_bf16 v[0:15], v[136:139], v[128:131], v[96:111]\n\t"
    "v_exp_f32 v21, v21\n\t"
    "v_exp_f32 v22, v22\n\t"
    "v_exp_f32 v23, v23\n\t"
    "v_exp_f32 v24, v24\n\t"
    "v_exp_f32 v25, v25\n\t"
    "v_exp_f32 v26, v26\n\t"
    "v_smfmac_f32_32x32x32_bf16 v[172:187], v[140:143], v[16:23], v219\n\t"
    "v_exp_f32 v27, v27\n\t"
    "v_exp_f32 v28, v28\n\t"
    "v_exp_f32 v29, v29\n\t"
    "v_exp_f32 v30, v30\n\t"
    "v_exp_f32 v31, v31\n\t"
    "s_waitcnt vmcnt(0)\n\t"
    "v_cmp_ne_u64 vcc, v[228:229], v[230:231]\n\t"
    "s_nop 4\n\t"
    "s_cbranch_vccnz 30f\n\t"
    "301:\n\t"
    "s_waitcnt lgkmcnt(0)\n\t"
    "s_barrier\n\t"
    "v_exp_f32 v0, v0\n\t"
    "v_exp_f32 v1, v1\n\t"
    "v_smfmac_f32_32x32x32_bf16 v[172:187], v[144:147], v[24:31], v219\n\t"
    "v_exp_f32 v2, v2\n\t"
    "v_exp_f32 v3, v3\n\t"
    "v_exp_f32 v4, v4\n\t"
    "v_mfma_f32_32x32x16_bf16 v[16:31], v[136:139], v[132:135], v[112:127]\n\t"
    "v_exp_f32 v5, v5\n\t"
    "s_mov_b64 exec, s[60:61]\n\t"
    "ds_read_b128 v[140:143], v205 offset:36864\n\t"
    "ds_read_b128 v[144:147], v205 offset:37376\n\t"
    "s_mov_b64 exec, -1\n\t"
    "v_exp_f32 v6, v6\n\t"
    "ds_read_b128 v[136:139], v204 offset:40960\n\t"
    "v_exp_f32 v7, v7\n\t"
    "v_exp_f32 v8, v8\n\t"
    "v_exp_f32 v9, v9\n\t"
    "v_exp_f32 v10, v10\n\t"
    "v_smfmac_f32_32x32x32_bf16 v[156:171], v[148:151], v[0:7], v219\n\t"
    "v_exp_f32 v11, v11\n\t"
    "v_exp_f32 v12, v12\n\t"
    "v_exp_f32 v13, v13\n\t"
    "v_exp_f32 v14, v14\n\t"
    "v_exp_f32 v15, v15\n\t"
    "v_exp_f32 v16, v16\n\t"
    "v_exp_f32 v17, v17\n\t"
    "v_smfmac_f32_32x32x32_bf16 v[156:171], v[152:155], v[8:15], v219\n\t"
    "v_exp_f32 v18, v18\n\t"
    "v_exp_f32 v19, v19\n\t"
    "s_waitcnt lgkmcnt(0)\n\t"
    "v_exp_f32 v20, v20\n\t"
    "v_mfma_f32_32x32x16_bf16 v[0:15], v[136:139], v[128:131], v[96:111]\n\t"
    "v_exp_f32 v21, v21\n\t"
    "v_exp_f32 v22, v22\n\t"
    "v_exp_f32 v23, v23\n\t"
    "v_exp_f32 v24, v24\n\t"
    "v_exp_f32 v25, v25\n\t"
    "v_exp_f32 v26, v26\n\t"
    "v_smfmac_f32_32x32x32_bf16 v[172:187], v[148:151], v[16:23], v219\n\t"
    "v_exp_f32 v27, v27\n\t"
    "v_exp_f32 v28, v28\n\t"
    "v_exp_f32 v29, v29\n\t"
    "v_exp_f32 v30, v30\n\t"
    "v_exp_f32 v31, v31\n\t"
    "s_waitcnt lgkmcnt(0)\n\t"
    "s_barrier\n\t"
    "v_exp_f32 v0, v0\n\t"
    "v_exp_f32 v1, v1\n\t"
    "v_smfmac_f32_32x32x32_bf16 v[172:187], v[152:155], v[24:31], v219\n\t"
    "v_exp_f32 v2, v2\n\t"
    "v_exp_f32 v3, v3\n\t"
    "v_exp_f32 v4, v4\n\t"
    "v_mfma_f32_32x32x16_bf16 v[16:31], v[136:139], v[132:135], v[112:127]\n\t"
    "v_exp_f32 v5, v5\n\t"
    "s_mov_b64 exec, s[60:61]\n\t"
    "ds_read_b128 v[148:151], v205 offset:0\n\t"
    "ds_read_b128 v[152:155], v205 offset:512\n\t"
    "s_mov_b64 exec, -1\n\t"
    "s_add_u32 m0, s83, 0\n\t"
    "v_exp_f32 v6, v6\n\t"
    "ds_read_b128 v[136:139], v204 offset:4096\n\t"
    "buffer_load_dwordx4 v232, s[84:87], 0 offen lds\n\t"
    "v_exp_f32 v7, v7\n\t"
    "v_exp_f32 v8, v8\n\t"
    "v_exp_f32 v9, v9\n\t"
    "v_exp_f32 v10, v10\n\t"
    "v_smfmac_f32_32x32x32_bf16 v[156:171], v[140:143], v[0:7], v219\n\t"
    "v_exp_f32 v11, v11\n\t"
    "v_exp_f32 v12, v12\n\t"
    "v_exp_f32 v13, v13\n\t"
    "buffer_load_dwordx4 v233, s[84:87], 0 offen offset:1024 lds\n\t"
    "v_exp_f32 v14, v14\n\t"
    "v_exp_f32 v15, v15\n\t"
    "v_exp_f32 v16, v16\n\t"
    "v_exp_f32 v17, v17\n\t"
    "v_smfmac_f32_32x32x32_bf16 v[156:171], v[144:147], v[8:15], v219\n\t"
    "v_exp_f32 v18, v18\n\t"
    "v_exp_f32 v19, v19\n\t"
    "s_waitcnt lgkmcnt(0)\n\t"
    "v_exp_f32 v20, v20\n\t"
    "v_mfma_f32_32x32x16_bf16 v[0:15], v[136:139], v[128:131], v[96:111]\n\t"
    "v_exp_f32 v21, v21\n\t"
    "v_exp_f32 v22, v22\n\t"
    "buffer_load_dwordx4 v234, s[84:87], 0 offen offset:2048 lds\n\t"
    "v_exp_f32 v23, v23\n\t"
    "v_exp_f32 v24, v24\n\t"
    "v_exp_f32 v25, v25\n\t"
    "v_exp_f32 v26, v26\n\t"
    "v_smfmac_f32_32x32x32_bf16 v[172:187], v[140:143], v[16:23], v219\n\t"
    "v_exp_f32 v27, v27\n\t"
    "v_exp_f32 v28, v28\n\t"
    "v_exp_f32 v29, v29\n\t"
    "buffer_load_dwordx4 v235, s[84:87], 0 offen offset:3072 lds\n\t"
    "v_exp_f32 v30, v30\n\t"
    "v_exp_f32 v31, v31\n\t"
    "s_waitcnt lgkmcnt(0)\n\t"
    "s_barrier\n\t"
    "v_exp_f32 v0, v0\n\t"
    "v_exp_f32 v1, v1\n\t"
    "v_smfmac_f32_32x32x32_bf16 v[172:187], v[144:147], v[24:31], v219\n\t"
    "v_exp_f32 v2, v2\n\t"
    "v_exp_f32 v3, v3\n\t"
    "v_exp_f32 v4, v4\n\t"
    "v_mfma_f32_32x32x16_bf16 v[16:31], v[136:139], v[132:135], v[112:127]\n\t"
    "v_exp_f32 v5, v5\n\t"
    "s_mov_b64 exec, s[60:61]\n\t"
    "ds_read_b128 v[140:143], v205 offset:12288\n\t"
    "ds_read_b128 v[144:147], v205 offset:12800\n\t"
    "s_mov_b64 exec, -1\n\t"
    "s_add_u32 m0, s83, 4096\n\t"
    "v_exp_f32 v6, v6\n\t"
    "ds_read_b128 v[136:139], v204 offset:16384\n\t"
    "buffer_load_dwordx4 v232, s[84:87], s82 offen lds\n\t"
    "v_exp_f32 v7, v7\n\t"
    "v_exp_f32 v8, v8\n\t"
    "ds_read_b128 v[56:59], v206 offset:0\n\t"
    "v_exp_f32 v9, v9\n\t"
    "ds_read_b128 v[48:51], v204 offset:12288\n\t"
    "v_exp_f32 v10, v10\n\t"
    "v_smfmac_f32_32x32x32_bf16 v[156:171], v[148:151], v[0:7], v219\n\t"
    "v_exp_f32 v11, v11\n\t"
    "ds_read_b128 v[52:55], v204 offset:13312\n\t"
    "v_exp_f32 v12, v12\n\t"
    "ds_read_b128 v[68:71], v207 offset:0\n\t"
    "v_exp_f32 v13, v13\n\t"
    "buffer_load_dwordx4 v233, s[84:87], s82 offen offset:1024 lds\n\t"
    "v_exp_f32 v14, v14\n\t"
    "ds_read_b128 v[60:63], v204 offset:14336\n\t"
    "v_exp_f32 v15, v15\n\t"
    "ds_read_b128 v[64:67], v204 offset:15360\n\t"
    "v_exp_f32 v16, v16\n\t"
    "v_exp_f32 v17, v17\n\t"
    "v_smfmac_f32_32x32x32_bf16 v[156:171], v[152:155], v[8:15], v219\n\t"
    "v_exp_f32 v18, v18\n\t"
    "v_exp_f32 v19, v19\n\t"
    "s_waitcnt lgkmcnt(6)\n\t"
    "v_exp_f32 v20, v20\n\t"
    "v_mfma_f32_32x32x16_bf16 v[0:15], v[136:139], v[128:131], v[96:111]\n\t"
    "v_exp_f32 v21, v21\n\t"
    "v_exp_f32 v22, v22\n\t"
    "buffer_load_dwordx4 v234, s[84:87], s82 offen offset:2048 lds\n\t"
    "v_exp_f32 v23, v23\n\t"
    "v_exp_f32 v24, v24\n\t"
    "v_exp_f32 v25, v25\n\t"
    "v_exp_f32 v26, v26\n\t"
    "v_smfmac_f32_32x32x32_bf16 v[172:187], v[148:151], v[16:23], v219\n\t"
    "v_exp_f32 v27, v27\n\t"
    "v_exp_f32 v28, v28\n\t"
    "v_exp_f32 v29, v29\n\t"
    "buffer_load_dwordx4 v235, s[84:87], s82 offen offset:3072 lds\n\t"
    "v_exp_f32 v30, v30\n\t"
    "v_exp_f32 v31, v31\n\t"
    "s_waitcnt lgkmcnt(0)\n\t"
    "s_movk_i32 s59, 2\n\t"
    ".p2align 6\n\t"
    "2:\n\t"
    "s_barrier\n\t"
    "s_setprio 0\n\t"
    "v_exp_f32 v0, v0\n\t"
    "v_exp_f32 v1, v1\n\t"
    "v_exp_f32 v2, v2\n\t"
    "v_exp_f32 v3, v3\n\t"
    "v_smfmac_f32_32x32x32_bf16 v[172:187], v[152:155], v[24:31], v219\n\t"
    "v_exp_f32 v4, v4\n\t"
    "v_mfma_f32_32x32x16_bf16 v[16:31], v[136:139], v[132:135], v[112:127]\n\t"
    "v_exp_f32 v5, v5\n\t"
    "s_mov_b64 exec, s[60:61]\n\t"
    "ds_read_b128 v[148:151], v205 offset:24576\n\t"
    "ds_read_b128 v[152:155], v205 offset:25088\n\t"
    "s_mov_b64 exec, -1\n\t"
    "v_exp_f32 v6, v6\n\t"
    "ds_read_b128 v[136:139], v204 offset:28672\n\t"
    "v_exp_f32 v7, v7\n\t"
    "v_exp_f32 v8, v8\n\t"
    "ds_read_b128 v[80:83], v208 offset:0\n\t"
    "v_exp_f32 v9, v9\n\t"
    "v_mfma_f32_32x32x16_bf16 v[188:203], v[48:51], v[56:59], v[188:203]\n\t"
    "ds_read_b128 v[72:75], v204 offset:24576\n\t"
    "v_exp_f32 v10, v10\n\t"
    "v_exp_f32 v11, v11\n\t"
    "ds_read_b128 v[76:79], v204 offset:25600\n\t"
    "v_exp_f32 v12, v12\n\t"
    "v_smfmac_f32_32x32x32_bf16 v[156:171], v[140:143], v[0:7], v219\n\t"
    "ds_read_b128 v[92:95], v209 offset:0\n\t"
    "v_exp_f32 v13, v13\n\t"
    "v_exp_f32 v14, v14\n\t"
    "ds_read_b128 v[84:87], v204 offset:26624\n\t"
    "v_exp_f32 v15, v15\n\t"
    "v_mfma_f32_32x32x16_bf16 v[238:253], v[52:55], v[56:59], v[238:253]\n\t"
    "ds_read_b128 v[88:91], v204 offset:27648\n\t"
    "v_exp_f32 v16, v16\n\t"
    "v_exp_f32 v17, v17\n\t"
    "v_exp_f32 v18, v18\n\t"
    "v_exp_f32 v19, v19\n\t"
    "v_smfmac_f32_32x32x32_bf16 v[156:171], v[144:147], v[8:15], v219\n\t"
    "s_waitcnt lgkmcnt(6)\n\t"
    "v_exp_f32 v20, v20\n\t"
    "v_mfma_f32_32x32x16_bf16 v[0:15], v[136:139], v[128:131], v[96:111]\n\t"
    "v_exp_f32 v21, v21\n\t"
    "v_exp_f32 v22, v22\n\t"
    "v_exp_f32 v23, v23\n\t"
    "v_exp_f32 v24, v24\n\t"
    "v_mfma_f32_32x32x16_bf16 v[188:203], v[60:63], v[68:71], v[188:203]\n\t"
    "s_setprio 2\n\t"
    "v_exp_f32 v25, v25\n\t"
    "v_exp_f32 v26, v26\n\t"
    "v_exp_f32 v27, v27\n\t"
    "v_exp_f32 v28, v28\n\t"
    "v_smfmac_f32_32x32x32_bf16 v[172:187], v[140:143], v[16:23], v219\n\t"
    "v_exp_f32 v29, v29\n\t"
    "v_exp_f32 v30, v30\n\t"
    "v_exp_f32 v31, v31\n\t"
    "v_mfma_f32_32x32x16_bf16 v[238:253], v[64:67], v[68:71], v[238:253]\n\t"
    "s_waitcnt lgkmcnt(0)\n\t"
    "s_barrier\n\t"
    "s_setprio 0\n\t"
    "v_exp_f32 v0, v0\n\t"
    "v_exp_f32 v1, v1\n\t"
    "v_exp_f32 v2, v2\n\t"
    "v_exp_f32 v3, v3\n\t"
    "v_smfmac_f32_32x32x32_bf16 v[172:187], v[144:147], v[24:31], v219\n\t"
    "v_exp_f32 v4, v4\n\t"
    "v_mfma_f32_32x32x16_bf16 v[16:31], v[136:139], v[132:135], v[112:127]\n\t"
    "v_exp_f32 v5, v5\n\t"
    "s_mov_b64 exec, s[60:61]\n\t"
    "ds_read_b128 v[140:143], v205 offset:36864\n\t"
    "ds_read_b128 v[144:147], v205 offset:37376\n\t"
    "s_mov_b64 exec, -1\n\t"
    "v_exp_f32 v6, v6\n\t"
    "ds_read_b128 v[136:139], v204 offset:40960\n\t"
    "s_cmp_eq_u32 s59, 2\n\t"
    "s_cbranch_scc1 52f\n\t"
    "62:\n\t"
    "v_exp_f32 v7, v7\n\t"
    "v_exp_f32 v8, v8\n\t"
    "ds_read_b128 v[56:59], v206 offset:16384\n\t"
    "v_exp_f32 v9, v9\n\t"
    "v_mfma_f32_32x32x16_bf16 v[188:203], v[72:75], v[80:83], v[188:203]\n\t"
    "ds_read_b128 v[48:51], v204 offset:36864\n\t"
    "v_exp_f32 v10, v10\n\t"
    "v_exp_f32 v11, v11\n\t"
    "ds_read_b128 v[52:55], v204 offset:37888\n\t"
    "v_exp_f32 v12, v12\n\t"
    "v_smfmac_f32_32x32x32_bf16 v[156:171], v[148:151], v[0:7], v219\n\t"
    "ds_read_b128 v[68:71], v207 offset:16384\n\t"
    "v_exp_f32 v13, v13\n\t"
    "v_exp_f32 v14, v14\n\t"
    "ds_read_b128 v[60:63], v204 offset:38912\n\t"
    "v_exp_f32 v15, v15\n\t"
    "v_mfma_f32_32x32x16_bf16 v[238:253], v[76:79], v[80:83], v[238:253]\n\t"
    "ds_read_b128 v[64:67], v204 offset:39936\n\t"
    "v_exp_f32 v16, v16\n\t"
    "v_exp_f32 v17, v17\n\t"
    "v_exp_f32 v18, v18\n\t"
    "v_exp_f32 v19, v19\n\t"
    "v_smfmac_f32_32x32x32_bf16 v[156:171], v[152:155], v[8:15], v219\n\t"
    "s_waitcnt lgkmcnt(6)\n\t"
    "v_exp_f32 v20, v20\n\t"
    "v_mfma_f32_32x32x16_bf16 v[0:15], v[136:139], v[128:131], v[96:111]\n\t"
    "v_exp_f32 v21, v21\n\t"
    "v_exp_f32 v22, v22\n\t"
    "v_exp_f32 v23, v23\n\t"
    "v_exp_f32 v24, v24\n\t"
    "v_mfma_f32_32x32x16_bf16 v[188:203], v[84:87], v[92:95], v[188:203]\n\t"
    "s_setprio 2\n\t"
    "v_exp_f32 v25, v25\n\t"
    "v_exp_f32 v26, v26\n\t"
    "v_exp_f32 v27, v27\n\t"
    "v_exp_f32 v28, v28\n\t"
    "v_smfmac_f32_32x32x32_bf16 v[172:187], v[148:151], v[16:23], v219\n\t"
    "v_exp_f32 v29, v29\n\t"
    "v_exp_f32 v30, v30\n\t"
    "v_exp_f32 v31, v31\n\t"
    "v_mfma_f32_32x32x16_bf16 v[238:253], v[88:91], v[92:95], v[238:253]\n\t"
    "s_waitcnt lgkmcnt(0)\n\t"
    "s_barrier\n\t"
    "s_setprio 0\n\t"
    "v_exp_f32 v0, v0\n\t"
    "v_exp_f32 v1, v1\n\t"
    "v_exp_f32 v2, v2\n\t"
    "v_exp_f32 v3, v3\n\t"
    "v_smfmac_f32_32x32x32_bf16 v[172:187], v[152:155], v[24:31], v219\n\t"
    "v_exp_f32 v4, v4\n\t"
    "v_mfma_f32_32x32x16_bf16 v[16:31], v[136:139], v[132:135], v[112:127]\n\t"
    "v_exp_f32 v5, v5\n\t"
    "s_mov_b64 exec, s[60:61]\n\t"
    "ds_read_b128 v[148:151], v205 offset:0\n\t"
    "ds_read_b128 v[152:155], v205 offset:512\n\t"
    "s_mov_b64 exec, -1\n\t"
    "v_exp_f32 v6, v6\n\t"
    "ds_read_b128 v[136:139], v204 offset:4096\n\t"
    "v_exp_f32 v7, v7\n\t"
    "v_exp_f32 v8, v8\n\t"
    "ds_read_b128 v[80:83], v208 offset:16384\n\t"
    "v_exp_f32 v9, v9\n\t"
    "v_mfma_f32_32x32x16_bf16 v[188:203], v[48:51], v[56:59], v[188:203]\n\t"
    "ds_read_b128 v[72:75], v204 offset:0\n\t"
    "v_exp_f32 v10, v10\n\t"
    "v_exp_f32 v11, v11\n\t"
    "ds_read_b128 v[76:79], v204 offset:1024\n\t"
    "v_exp_f32 v12, v12\n\t"
    "v_smfmac_f32_32x32x32_bf16 v[156:171], v[140:143], v[0:7], v219\n\t"
    "ds_read_b128 v[92:95], v209 offset:16384\n\t"
    "v_exp_f32 v13, v13\n\t"
    "v_exp_f32 v14, v14\n\t"
    "ds_read_b128 v[84:87], v204 offset:2048\n\t"
    "v_exp_f32 v15, v15\n\t"
    "v_mfma_f32_32x32x16_bf16 v[238:253], v[52:55], v[56:59], v[238:253]\n\t"
    "ds_read_b128 v[88:91], v204 offset:3072\n\t"
    "v_exp_f32 v16, v16\n\t"
    "v_exp_f32 v17, v17\n\t"
    "v_exp_f32 v18, v18\n\t"
    "v_exp_f32 v19, v19\n\t"
    "v_smfmac_f32_32x32x32_bf16 v[156:171], v[144:147], v[8:15], v219\n\t"
    "s_waitcnt lgkmcnt(6)\n\t"
    "v_exp_f32 v20, v20\n\t"
    "v_mfma_f32_32x32x16_bf16 v[0:15], v[136:139], v[128:131], v[96:111]\n\t"
    "v_exp_f32 v21, v21\n\t"
    "v_exp_f32 v22, v22\n\t"
    "v_exp_f32 v23, v23\n\t"
    "v_exp_f32 v24, v24\n\t"
    "v_mfma_f32_32x32x16_bf16 v[188:203], v[60:63], v[68:71], v[188:203]\n\t"
    "s_setprio 2\n\t"
    "v_exp_f32 v25, v25\n\t"
    "v_exp_f32 v26, v26\n\t"
    "v_exp_f32 v27, v27\n\t"
    "v_exp_f32 v28, v28\n\t"
    "v_smfmac_f32_32x32x32_bf16 v[172:187], v[140:143], v[16:23], v219\n\t"
    "v_exp_f32 v29, v29\n\t"
    "v_exp_f32 v30, v30\n\t"
    "v_exp_f32 v31, v31\n\t"
    "v_mfma_f32_32x32x16_bf16 v[238:253], v[64:67], v[68:71], v[238:253]\n\t"
    "s_waitcnt lgkmcnt(0)\n\t"
    "s_barrier\n\t"
    "s_setprio 0\n\t"
    "v_exp_f32 v0, v0\n\t"
    "v_exp_f32 v1, v1\n\t"
    "v_exp_f32 v2, v2\n\t"
    "v_exp_f32 v3, v3\n\t"
    "v_smfmac_f32_32x32x32_bf16 v[172:187], v[144:147], v[24:31], v219\n\t"
    "v_exp_f32 v4, v4\n\t"
    "v_mfma_f32_32x32x16_bf16 v[16:31], v[136:139], v[132:135], v[112:127]\n\t"
    "v_exp_f32 v5, v5\n\t"
    "s_mov_b64 exec, s[60:61]\n\t"
    "ds_read_b128 v[140:143], v205 offset:12288\n\t"
    "ds_read_b128 v[144:147], v205 offset:12800\n\t"
    "s_mov_b64 exec, -1\n\t"
    "v_exp_f32 v6, v6\n\t"
    "ds_read_b128 v[136:139], v204 offset:16384\n\t"
    "v_exp_f32 v7, v7\n\t"
    "v_exp_f32 v8, v8\n\t"
    "ds_read_b128 v[56:59], v206 offset:32768\n\t"
    "v_exp_f32 v9, v9\n\t"
    "v_mfma_f32_32x32x16_bf16 v[188:203], v[72:75], v[80:83], v[188:203]\n\t"
    "ds_read_b128 v[48:51], v204 offset:12288\n\t"
    "v_exp_f32 v10, v10\n\t"
    "v_exp_f32 v11, v11\n\t"
    "ds_read_b128 v[52:55], v204 offset:13312\n\t"
    "v_exp_f32 v12, v12\n\t"
    "v_smfmac_f32_32x32x32_bf16 v[156:171], v[148:151], v[0:7], v219\n\t"
    "ds_read_b128 v[68:71], v207 offset:32768\n\t"
    "v_exp_f32 v13, v13\n\t"
    "v_exp_f32 v14, v14\n\t"
    "ds_read_b128 v[60:63], v204 offset:14336\n\t"
    "v_exp_f32 v15, v15\n\t"
    "v_mfma_f32_32x32x16_bf16 v[238:253], v[76:79], v[80:83], v[238:253]\n\t"
    "ds_read_b128 v[64:67], v204 offset:15360\n\t"
    "v_exp_f32 v16, v16\n\t"
    "v_exp_f32 v17, v17\n\t"
    "v_exp_f32 v18, v18\n\t"
    "v_exp_f32 v19, v19\n\t"
    "v_smfmac_f32_32x32x32_bf16 v[156:171], v[152:155], v[8:15], v219\n\t"
    "s_waitcnt lgkmcnt(6)\n\t"
    "v_exp_f32 v20, v20\n\t"
    "v_mfma_f32_32x32x16_bf16 v[0:15], v[136:139], v[128:131], v[96:111]\n\t"
    "v_exp_f32 v21, v21\n\t"
    "v_exp_f32 v22, v22\n\t"
    "v_exp_f32 v23, v23\n\t"
    "v_exp_f32 v24, v24\n\t"
    "v_mfma_f32_32x32x16_bf16 v[188:203], v[84:87], v[92:95], v[188:203]\n\t"
    "s_setprio 2\n\t"
    "v_exp_f32 v25, v25\n\t"
    "v_exp_f32 v26, v26\n\t"
    "v_exp_f32 v27, v27\n\t"
    "v_exp_f32 v28, v28\n\t"
    "v_smfmac_f32_32x32x32_bf16 v[172:187], v[148:151], v[16:23], v219\n\t"
    "v_exp_f32 v29, v29\n\t"
    "v_exp_f32 v30, v30\n\t"
    "v_exp_f32 v31, v31\n\t"
    "v_mfma_f32_32x32x16_bf16 v[238:253], v[88:91], v[92:95], v[238:253]\n\t"
    "s_waitcnt lgkmcnt(0)\n\t"
    "s_barrier\n\t"
    "s_setprio 0\n\t"
    "v_exp_f32 v0, v0\n\t"
    "v_exp_f32 v1, v1\n\t"
    "v_exp_f32 v2, v2\n\t"
    "v_exp_f32 v3, v3\n\t"
    "v_smfmac_f32_32x32x32_bf16 v[172:187], v[152:155], v[24:31], v219\n\t"
    "v_exp_f32 v4, v4\n\t"
    "v_mfma_f32_32x32x16_bf16 v[16:31], v[136:139], v[132:135], v[112:127]\n\t"
    "v_exp_f32 v5, v5\n\t"
    "s_mov_b64 exec, s[60:61]\n\t"
    "ds_read_b128 v[148:151], v205 offset:24576\n\t"
    "ds_read_b128 v[152:155], v205 offset:25088\n\t"
    "s_mov_b64 exec, -1\n\t"
    "v_exp_f32 v6, v6\n\t"
    "ds_read_b128 v[136:139], v204 offset:28672\n\t"
    "v_exp_f32 v7, v7\n\t"
    "v_exp_f32 v8, v8\n\t"
    "ds_read_b128 v[80:83], v208 offset:32768\n\t"
    "v_exp_f32 v9, v9\n\t"
    "v_mfma_f32_32x32x16_bf16 v[188:203], v[48:51], v[56:59], v[188:203]\n\t"
    "ds_read_b128 v[72:75], v204 offset:24576\n\t"
    "v_exp_f32 v10, v10\n\t"
    "v_exp_f32 v11, v11\n\t"
    "ds_read_b128 v[76:79], v204 offset:25600\n\t"
    "v_exp_f32 v12, v12\n\t"
    "v_smfmac_f32_32x32x32_bf16 v[156:171], v[140:143], v[0:7], v219\n\t"
    "ds_read_b128 v[92:95], v209 offset:32768\n\t"
    "v_exp_f32 v13, v13\n\t"
    "v_exp_f32 v14, v14\n\t"
    "ds_read_b128 v[84:87], v204 offset:26624\n\t"
    "v_exp_f32 v15, v15\n\t"
    "v_mfma_f32_32x32x16_bf16 v[238:253], v[52:55], v[56:59], v[238:253]\n\t"
    "ds_read_b128 v[88:91], v204 offset:27648\n\t"
    "v_exp_f32 v16, v16\n\t"
    "v_exp_f32 v17, v17\n\t"
    "v_exp_f32 v18, v18\n\t"
    "v_exp_f32 v19, v19\n\t"
    "v_smfmac_f32_32x32x32_bf16 v[156:171], v[144:147], v[8:15], v219\n\t"
    "s_waitcnt lgkmcnt(6)\n\t"
    "v_exp_f32 v20, v20\n\t"
    "v_mfma_f32_32x32x16_bf16 v[0:15], v[136:139], v[128:131], v[96:111]\n\t"
    "v_exp_f32 v21, v21\n\t"
    "v_exp_f32 v22, v22\n\t"
    "v_exp_f32 v23, v23\n\t"
    "v_exp_f32 v24, v24\n\t"
    "v_mfma_f32_32x32x16_bf16 v[188:203], v[60:63], v[68:71], v[188:203]\n\t"
    "s_setprio 2\n\t"
    "v_exp_f32 v25, v25\n\t"
    "v_exp_f32 v26, v26\n\t"
    "v_exp_f32 v27, v27\n\t"
    "v_exp_f32 v28, v28\n\t"
    "v_smfmac_f32_32x32x32_bf16 v[172:187], v[140:143], v[16:23], v219\n\t"
    "v_exp_f32 v29, v29\n\t"
    "v_exp_f32 v30, v30\n\t"
    "v_exp_f32 v31, v31\n\t"
    "v_mfma_f32_32x32x16_bf16 v[238:253], v[64:67], v[68:71], v[238:253]\n\t"
    "s_waitcnt lgkmcnt(0)\n\t"
    "s_barrier\n\t"
    "s_setprio 0\n\t"
    "v_exp_f32 v0, v0\n\t"
    "v_exp_f32 v1, v1\n\t"
    "v_exp_f32 v2, v2\n\t"
    "v_exp_f32 v3, v3\n\t"
    "v_smfmac_f32_32x32x32_bf16 v[172:187], v[144:147], v[24:31], v219\n\t"
    "v_exp_f32 v4, v4\n\t"
    "v_mfma_f32_32x32x16_bf16 v[16:31], v[136:139], v[132:135], v[112:127]\n\t"
    "v_exp_f32 v5, v5\n\t"
    "s_mov_b64 exec, s[60:61]\n\t"
    "ds_read_b128 v[140:143], v205 offset:36864\n\t"
    "ds_read_b128 v[144:147], v205 offset:37376\n\t"
    "s_mov_b64 exec, -1\n\t"
    "v_exp_f32 v6, v6\n\t"
    "ds_read_b128 v[136:139], v204 offset:40960\n\t"
    "v_exp_f32 v7, v7\n\t"
    "v_exp_f32 v8, v8\n\t"
    "ds_read_b128 v[56:59], v206 offset:0\n\t"
    "v_exp_f32 v9, v9\n\t"
    "v_mfma_f32_32x32x16_bf16 v[188:203], v[72:75], v[80:83], v[188:203]\n\t"
    "ds_read_b128 v[48:51], v204 offset:36864\n\t"
    "v_exp_f32 v10, v10\n\t"
    "v_exp_f32 v11, v11\n\t"
    "ds_read_b128 v[52:55], v204 offset:37888\n\t"
    "v_exp_f32 v12, v12\n\t"
    "v_smfmac_f32_32x32x32_bf16 v[156:171], v[148:151], v[0:7], v219\n\t"
    "ds_read_b128 v[68:71], v207 offset:0\n\t"
    "v_exp_f32 v13, v13\n\t"
    "v_exp_f32 v14, v14\n\t"
    "ds_read_b128 v[60:63], v204 offset:38912\n\t"
    "v_exp_f32 v15, v15\n\t"
    "v_mfma_f32_32x32x16_bf16 v[238:253], v[76:79], v[80:83], v[238:253]\n\t"
    "ds_read_b128 v[64:67], v204 offset:39936\n\t"
    "v_exp_f32 v16, v16\n\t"
    "v_exp_f32 v17, v17\n\t"
    "v_exp_f32 v18, v18\n\t"
    "v_exp_f32 v19, v19\n\t"
    "v_smfmac_f32_32x32x32_bf16 v[156:171], v[152:155], v[8:15], v219\n\t"
    "s_waitcnt lgkmcnt(6)\n\t"
    "v_exp_f32 v20, v20\n\t"
    "v_mfma_f32_32x32x16_bf16 v[0:15], v[136:139], v[128:131], v[96:111]\n\t"
    "v_exp_f32 v21, v21\n\t"
    "v_exp_f32 v22, v22\n\t"
    "v_exp_f32 v23, v23\n\t"
    "v_exp_f32 v24, v24\n\t"
    "v_mfma_f32_32x32x16_bf16 v[188:203], v[84:87], v[92:95], v[188:203]\n\t"
    "s_setprio 2\n\t"
    "v_exp_f32 v25, v25\n\t"
    "v_exp_f32 v26, v26\n\t"
    "v_exp_f32 v27, v27\n\t"
    "v_exp_f32 v28, v28\n\t"
    "v_smfmac_f32_32x32x32_bf16 v[172:187], v[148:151], v[16:23], v219\n\t"
    "v_exp_f32 v29, v29\n\t"
    "v_exp_f32 v30, v30\n\t"
    "v_exp_f32 v31, v31\n\t"
    "v_mfma_f32_32x32x16_bf16 v[238:253], v[88:91], v[92:95], v[238:253]\n\t"
    "s_waitcnt lgkmcnt(0)\n\t"
    "s_barrier\n\t"
    "s_setprio 0\n\t"
    "v_exp_f32 v0, v0\n\t"
    "v_exp_f32 v1, v1\n\t"
    "v_exp_f32 v2, v2\n\t"
    "v_exp_f32 v3, v3\n\t"
    "v_smfmac_f32_32x32x32_bf16 v[172:187], v[152:155], v[24:31], v219\n\t"
    "v_exp_f32 v4, v4\n\t"
    "v_mfma_f32_32x32x16_bf16 v[16:31], v[136:139], v[132:135], v[112:127]\n\t"
    "v_exp_f32 v5, v5\n\t"
    "s_mov_b64 exec, s[60:61]\n\t"
    "ds_read_b128 v[148:151], v205 offset:0\n\t"
    "ds_read_b128 v[152:155], v205 offset:512\n\t"
    "s_mov_b64 exec, -1\n\t"
    "v_exp_f32 v6, v6\n\t"
    "ds_read_b128 v[136:139], v204 offset:4096\n\t"
    "v_exp_f32 v7, v7\n\t"
    "v_exp_f32 v8, v8\n\t"
    "ds_read_b128 v[80:83], v208 offset:0\n\t"
    "v_exp_f32 v9, v9\n\t"
    "v_mfma_f32_32x32x16_bf16 v[188:203], v[48:51], v[56:59], v[188:203]\n\t"
    "ds_read_b128 v[72:75], v204 offset:0\n\t"
    "v_exp_f32 v10, v10\n\t"
    "v_exp_f32 v11, v11\n\t"
    "ds_read_b128 v[76:79], v204 offset:1024\n\t"
    "v_exp_f32 v12, v12\n\t"
    "v_smfmac_f32_32x32x32_bf16 v[156:171], v[140:143], v[0:7], v219\n\t"
    "ds_read_b128 v[92:95], v209 offset:0\n\t"
    "v_exp_f32 v13, v13\n\t"
    "v_exp_f32 v14, v14\n\t"
    "ds_read_b128 v[84:87], v204 offset:2048\n\t"
    "v_exp_f32 v15, v15\n\t"
    "v_mfma_f32_32x32x16_bf16 v[238:253], v[52:55], v[56:59], v[238:253]\n\t"
    "ds_read_b128 v[88:91], v204 offset:3072\n\t"
    "v_exp_f32 v16, v16\n\t"
    "v_exp_f32 v17, v17\n\t"
    "v_exp_f32 v18, v18\n\t"
    "v_exp_f32 v19, v19\n\t"
    "v_smfmac_f32_32x32x32_bf16 v[156:171], v[144:147], v[8:15], v219\n\t"
    "s_waitcnt lgkmcnt(6)\n\t"
    "v_exp_f32 v20, v20\n\t"
    "v_mfma_f32_32x32x16_bf16 v[0:15], v[136:139], v[128:131], v[96:111]\n\t"
    "v_exp_f32 v21, v21\n\t"
    "v_exp_f32 v22, v22\n\t"
    "v_exp_f32 v23, v23\n\t"
    "v_exp_f32 v24, v24\n\t"
    "v_mfma_f32_32x32x16_bf16 v[188:203], v[60:63], v[68:71], v[188:203]\n\t"
    "s_setprio 2\n\t"
    "v_exp_f32 v25, v25\n\t"
    "v_exp_f32 v26, v26\n\t"
    "v_exp_f32 v27, v27\n\t"
    "v_exp_f32 v28, v28\n\t"
    "v_smfmac_f32_32x32x32_bf16 v[172:187], v[140:143], v[16:23], v219\n\t"
    "v_exp_f32 v29, v29\n\t"
    "v_exp_f32 v30, v30\n\t"
    "v_exp_f32 v31, v31\n\t"
    "v_mfma_f32_32x32x16_bf16 v[238:253], v[64:67], v[68:71], v[238:253]\n\t"
    "s_waitcnt lgkmcnt(0)\n\t"
    "s_barrier\n\t"
    "s_setprio 0\n\t"
    "v_exp_f32 v0, v0\n\t"
    "v_exp_f32 v1, v1\n\t"
    "v_exp_f32 v2, v2\n\t"
    "v_exp_f32 v3, v3\n\t"
    "v_smfmac_f32_32x32x32_bf16 v[172:187], v[144:147], v[24:31], v219\n\t"
    "v_exp_f32 v4, v4\n\t"
    "v_mfma_f32_32x32x16_bf16 v[16:31], v[136:139], v[132:135], v[112:127]\n\t"
    "v_exp_f32 v5, v5\n\t"
    "s_mov_b64 exec, s[60:61]\n\t"
    "ds_read_b128 v[140:143], v205 offset:12288\n\t"
    "ds_read_b128 v[144:147], v205 offset:12800\n\t"
    "s_mov_b64 exec, -1\n\t"
    "v_exp_f32 v6, v6\n\t"
    "ds_read_b128 v[136:139], v204 offset:16384\n\t"
    "v_exp_f32 v7, v7\n\t"
    "v_exp_f32 v8, v8\n\t"
    "ds_read_b128 v[56:59], v206 offset:16384\n\t"
    "v_exp_f32 v9, v9\n\t"
    "v_mfma_f32_32x32x16_bf16 v[188:203], v[72:75], v[80:83], v[188:203]\n\t"
    "ds_read_b128 v[48:51], v204 offset:12288\n\t"
    "v_exp_f32 v10, v10\n\t"
    "v_exp_f32 v11, v11\n\t"
    "ds_read_b128 v[52:55], v204 offset:13312\n\t"
    "v_exp_f32 v12, v12\n\t"
    "v_smfmac_f32_32x32x32_bf16 v[156:171], v[148:151], v[0:7], v219\n\t"
    "ds_read_b128 v[68:71], v207 offset:16384\n\t"
    "v_exp_f32 v13, v13\n\t"
    "v_exp_f32 v14, v14\n\t"
    "ds_read_b128 v[60:63], v204 offset:14336\n\t"
    "v_exp_f32 v15, v15\n\t"
    "v_mfma_f32_32x32x16_bf16 v[238:253], v[76:79], v[80:83], v[238:253]\n\t"
    "ds_read_b128 v[64:67], v204 offset:15360\n\t"
    "v_exp_f32 v16, v16\n\t"
    "v_exp_f32 v17, v17\n\t"
    "v_exp_f32 v18, v18\n\t"
    "v_exp_f32 v19, v19\n\t"
    "v_smfmac_f32_32x32x32_bf16 v[156:171], v[152:155], v[8:15], v219\n\t"
    "s_waitcnt lgkmcnt(6)\n\t"
    "v_exp_f32 v20, v20\n\t"
    "v_mfma_f32_32x32x16_bf16 v[0:15], v[136:139], v[128:131], v[96:111]\n\t"
    "v_exp_f32 v21, v21\n\t"
    "v_exp_f32 v22, v22\n\t"
    "v_exp_f32 v23, v23\n\t"
    "v_exp_f32 v24, v24\n\t"
    "v_mfma_f32_32x32x16_bf16 v[188:203], v[84:87], v[92:95], v[188:203]\n\t"
    "s_setprio 2\n\t"
    "v_exp_f32 v25, v25\n\t"
    "v_exp_f32 v26, v26\n\t"
    "v_exp_f32 v27, v27\n\t"
    "v_exp_f32 v28, v28\n\t"
    "v_smfmac_f32_32x32x32_bf16 v[172:187], v[148:151], v[16:23], v219\n\t"
    "v_exp_f32 v29, v29\n\t"
    "v_exp_f32 v30, v30\n\t"
    "v_exp_f32 v31, v31\n\t"
    "v_mfma_f32_32x32x16_bf16 v[238:253], v[88:91], v[92:95], v[238:253]\n\t"
    "s_waitcnt lgkmcnt(0)\n\t"
    "s_barrier\n\t"
    "s_setprio 0\n\t"
    "v_exp_f32 v0, v0\n\t"
    "v_exp_f32 v1, v1\n\t"
    "v_exp_f32 v2, v2\n\t"
    "v_exp_f32 v3, v3\n\t"
    "v_smfmac_f32_32x32x32_bf16 v[172:187], v[152:155], v[24:31], v219\n\t"
    "v_exp_f32 v4, v4\n\t"
    "v_mfma_f32_32x32x16_bf16 v[16:31], v[136:139], v[132:135], v[112:127]\n\t"
    "v_exp_f32 v5, v5\n\t"
    "s_mov_b64 exec, s[60:61]\n\t"
    "ds_read_b128 v[148:151], v205 offset:24576\n\t"
    "ds_read_b128 v[152:155], v205 offset:25088\n\t"
    "s_mov_b64 exec, -1\n\t"
    "v_exp_f32 v6, v6\n\t"
    "ds_read_b128 v[136:139], v204 offset:28672\n\t"
    "v_exp_f32 v7, v7\n\t"
    "v_exp_f32 v8, v8\n\t"
    "ds_read_b128 v[80:83], v208 offset:16384\n\t"
    "v_exp_f32 v9, v9\n\t"
    "v_mfma_f32_32x32x16_bf16 v[188:203], v[48:51], v[56:59], v[188:203]\n\t"
    "ds_read_b128 v[72:75], v204 offset:24576\n\t"
    "v_exp_f32 v10, v10\n\t"
    "v_exp_f32 v11, v11\n\t"
    "ds_read_b128 v[76:79], v204 offset:25600\n\t"
    "v_exp_f32 v12, v12\n\t"
    "v_smfmac_f32_32x32x32_bf16 v[156:171], v[140:143], v[0:7], v219\n\t"
    "ds_read_b128 v[92:95], v209 offset:16384\n\t"
    "v_exp_f32 v13, v13\n\t"
    "v_exp_f32 v14, v14\n\t"
    "ds_read_b128 v[84:87], v204 offset:26624\n\t"
    "v_exp_f32 v15, v15\n\t"
    "v_mfma_f32_32x32x16_bf16 v[238:253], v[52:55], v[56:59], v[238:253]\n\t"
    "ds_read_b128 v[88:91], v204 offset:27648\n\t"
    "v_exp_f32 v16, v16\n\t"
    "v_exp_f32 v17, v17\n\t"
    "v_exp_f32 v18, v18\n\t"
    "v_exp_f32 v19, v19\n\t"
    "v_smfmac_f32_32x32x32_bf16 v[156:171], v[144:147], v[8:15], v219\n\t"
    "s_waitcnt lgkmcnt(6)\n\t"
    "v_exp_f32 v20, v20\n\t"
    "v_mfma_f32_32x32x16_bf16 v[0:15], v[136:139], v[128:131], v[96:111]\n\t"
    "v_exp_f32 v21, v21\n\t"
    "v_exp_f32 v22, v22\n\t"
    "v_exp_f32 v23, v23\n\t"
    "v_exp_f32 v24, v24\n\t"
    "v_mfma_f32_32x32x16_bf16 v[188:203], v[60:63], v[68:71], v[188:203]\n\t"
    "s_setprio 2\n\t"
    "v_exp_f32 v25, v25\n\t"
    "v_exp_f32 v26, v26\n\t"
    "v_exp_f32 v27, v27\n\t"
    "v_exp_f32 v28, v28\n\t"
    "v_smfmac_f32_32x32x32_bf16 v[172:187], v[140:143], v[16:23], v219\n\t"
    "v_exp_f32 v29, v29\n\t"
    "v_exp_f32 v30, v30\n\t"
    "v_exp_f32 v31, v31\n\t"
    "v_mfma_f32_32x32x16_bf16 v[238:253], v[64:67], v[68:71], v[238:253]\n\t"
    "s_waitcnt lgkmcnt(0)\n\t"
    "s_barrier\n\t"
    "s_setprio 0\n\t"
    "v_exp_f32 v0, v0\n\t"
    "v_exp_f32 v1, v1\n\t"
    "v_exp_f32 v2, v2\n\t"
    "v_exp_f32 v3, v3\n\t"
    "v_smfmac_f32_32x32x32_bf16 v[172:187], v[144:147], v[24:31], v219\n\t"
    "v_exp_f32 v4, v4\n\t"
    "v_mfma_f32_32x32x16_bf16 v[16:31], v[136:139], v[132:135], v[112:127]\n\t"
    "v_exp_f32 v5, v5\n\t"
    "s_mov_b64 exec, s[60:61]\n\t"
    "ds_read_b128 v[140:143], v205 offset:36864\n\t"
    "ds_read_b128 v[144:147], v205 offset:37376\n\t"
    "s_mov_b64 exec, -1\n\t"
    "v_exp_f32 v6, v6\n\t"
    "ds_read_b128 v[136:139], v204 offset:40960\n\t"
    "v_exp_f32 v7, v7\n\t"
    "v_exp_f32 v8, v8\n\t"
    "ds_read_b128 v[56:59], v206 offset:32768\n\t"
    "v_exp_f32 v9, v9\n\t"
    "v_mfma_f32_32x32x16_bf16 v[188:203], v[72:75], v[80:83], v[188:203]\n\t"
    "ds_read_b128 v[48:51], v204 offset:36864\n\t"
    "v_exp_f32 v10, v10\n\t"
    "v_exp_f32 v11, v11\n\t"
    "ds_read_b128 v[52:55], v204 offset:37888\n\t"
    "v_exp_f32 v12, v12\n\t"
    "v_smfmac_f32_32x32x32_bf16 v[156:171], v[148:151], v[0:7], v219\n\t"
    "ds_read_b128 v[68:71], v207 offset:32768\n\t"
    "v_exp_f32 v13, v13\n\t"
    "v_exp_f32 v14, v14\n\t"
    "ds_read_b128 v[60:63], v204 offset:38912\n\t"
    "v_exp_f32 v15, v15\n\t"
    "v_mfma_f32_32x32x16_bf16 v[238:253], v[76:79], v[80:83], v[238:253]\n\t"
    "ds_read_b128 v[64:67], v204 offset:39936\n\t"
    "v_exp_f32 v16, v16\n\t"
    "v_exp_f32 v17, v17\n\t"
    "v_exp_f32 v18, v18\n\t"
    "v_exp_f32 v19, v19\n\t"
    "v_smfmac_f32_32x32x32_bf16 v[156:171], v[152:155], v[8:15], v219\n\t"
    "s_waitcnt lgkmcnt(6)\n\t"
    "v_exp_f32 v20, v20\n\t"
    "v_mfma_f32_32x32x16_bf16 v[0:15], v[136:139], v[128:131], v[96:111]\n\t"
    "v_exp_f32 v21, v21\n\t"
    "v_exp_f32 v22, v22\n\t"
    "v_exp_f32 v23, v23\n\t"
    "v_exp_f32 v24, v24\n\t"
    "v_mfma_f32_32x32x16_bf16 v[188:203], v[84:87], v[92:95], v[188:203]\n\t"
    "s_setprio 2\n\t"
    "v_exp_f32 v25, v25\n\t"
    "v_exp_f32 v26, v26\n\t"
    "v_exp_f32 v27, v27\n\t"
    "v_exp_f32 v28, v28\n\t"
    "v_smfmac_f32_32x32x32_bf16 v[172:187], v[148:151], v[16:23], v219\n\t"
    "v_exp_f32 v29, v29\n\t"
    "v_exp_f32 v30, v30\n\t"
    "v_exp_f32 v31, v31\n\t"
    "v_mfma_f32_32x32x16_bf16 v[238:253], v[88:91], v[92:95], v[238:253]\n\t"
    "s_waitcnt lgkmcnt(0)\n\t"
    "s_barrier\n\t"
    "s_setprio 0\n\t"
    "v_exp_f32 v0, v0\n\t"
    "v_exp_f32 v1, v1\n\t"
    "v_exp_f32 v2, v2\n\t"
    "v_exp_f32 v3, v3\n\t"
    "v_smfmac_f32_32x32x32_bf16 v[172:187], v[152:155], v[24:31], v219\n\t"
    "v_exp_f32 v4, v4\n\t"
    "v_mfma_f32_32x32x16_bf16 v[16:31], v[136:139], v[132:135], v[112:127]\n\t"
    "v_exp_f32 v5, v5\n\t"
    "s_mov_b64 exec, s[60:61]\n\t"
    "ds_read_b128 v[148:151], v205 offset:0\n\t"
    "ds_read_b128 v[152:155], v205 offset:512\n\t"
    "s_mov_b64 exec, -1\n\t"
    "v_exp_f32 v6, v6\n\t"
    "ds_read_b128 v[136:139], v204 offset:4096\n\t"
    "v_exp_f32 v7, v7\n\t"
    "v_exp_f32 v8, v8\n\t"
    "ds_read_b128 v[80:83], v208 offset:32768\n\t"
    "v_exp_f32 v9, v9\n\t"
    "v_mfma_f32_32x32x16_bf16 v[188:203], v[48:51], v[56:59], v[188:203]\n\t"
    "ds_read_b128 v[72:75], v204 offset:0\n\t"
    "v_exp_f32 v10, v10\n\t"
    "v_exp_f32 v11, v11\n\t"
    "ds_read_b128 v[76:79], v204 offset:1024\n\t"
    "v_exp_f32 v12, v12\n\t"
    "v_smfmac_f32_32x32x32_bf16 v[156:171], v[140:143], v[0:7], v219\n\t"
    "ds_read_b128 v[92:95], v209 offset:32768\n\t"
    "v_exp_f32 v13, v13\n\t"
    "v_exp_f32 v14, v14\n\t"
    "ds_read_b128 v[84:87], v204 offset:2048\n\t"
    "v_exp_f32 v15, v15\n\t"
    "v_mfma_f32_32x32x16_bf16 v[238:253], v[52:55], v[56:59], v[238:253]\n\t"
    "ds_read_b128 v[88:91], v204 offset:3072\n\t"
    "v_exp_f32 v16, v16\n\t"
    "v_exp_f32 v17, v17\n\t"
    "v_exp_f32 v18, v18\n\t"
    "v_exp_f32 v19, v19\n\t"
    "v_smfmac_f32_32x32x32_bf16 v[156:171], v[144:147], v[8:15], v219\n\t"
    "s_waitcnt lgkmcnt(6)\n\t"
    "v_exp_f32 v20, v20\n\t"
    "v_mfma_f32_32x32x16_bf16 v[0:15], v[136:139], v[128:131], v[96:111]\n\t"
    "v_exp_f32 v21, v21\n\t"
    "v_exp_f32 v22, v22\n\t"
    "v_exp_f32 v23, v23\n\t"
    "v_exp_f32 v24, v24\n\t"
    "v_mfma_f32_32x32x16_bf16 v[188:203], v[60:63], v[68:71], v[188:203]\n\t"
    "s_setprio 2\n\t"
    "v_exp_f32 v25, v25\n\t"
    "v_exp_f32 v26, v26\n\t"
    "v_exp_f32 v27, v27\n\t"
    "v_exp_f32 v28, v28\n\t"
    "v_smfmac_f32_32x32x32_bf16 v[172:187], v[140:143], v[16:23], v219\n\t"
    "v_exp_f32 v29, v29\n\t"
    "v_exp_f32 v30, v30\n\t"
    "v_exp_f32 v31, v31\n\t"
    "v_mfma_f32_32x32x16_bf16 v[238:253], v[64:67], v[68:71], v[238:253]\n\t"
    "s_waitcnt lgkmcnt(0)\n\t"
    "s_barrier\n\t"
    "s_setprio 0\n\t"
    "v_exp_f32 v0, v0\n\t"
    "v_exp_f32 v1, v1\n\t"
    "v_exp_f32 v2, v2\n\t"
    "v_exp_f32 v3, v3\n\t"
    "v_smfmac_f32_32x32x32_bf16 v[172:187], v[144:147], v[24:31], v219\n\t"
    "v_exp_f32 v4, v4\n\t"
    "v_mfma_f32_32x32x16_bf16 v[16:31], v[136:139], v[132:135], v[112:127]\n\t"
    "v_exp_f32 v5, v5\n\t"
    "s_mov_b64 exec, s[60:61]\n\t"
    "ds_read_b128 v[140:143], v205 offset:12288\n\t"
    "ds_read_b128 v[144:147], v205 offset:12800\n\t"
    "s_mov_b64 exec, -1\n\t"
    "v_exp_f32 v6, v6\n\t"
    "ds_read_b128 v[136:139], v204 offset:16384\n\t"
    "v_exp_f32 v7, v7\n\t"
    "v_exp_f32 v8, v8\n\t"
    "ds_read_b128 v[56:59], v206 offset:0\n\t"
    "v_exp_f32 v9, v9\n\t"
    "v_mfma_f32_32x32x16_bf16 v[188:203], v[72:75], v[80:83], v[188:203]\n\t"
    "ds_read_b128 v[48:51], v204 offset:12288\n\t"
    "v_exp_f32 v10, v10\n\t"
    "v_exp_f32 v11, v11\n\t"
    "ds_read_b128 v[52:55], v204 offset:13312\n\t"
    "v_exp_f32 v12, v12\n\t"
    "v_smfmac_f32_32x32x32_bf16 v[156:171], v[148:151], v[0:7], v219\n\t"
    "ds_read_b128 v[68:71], v207 offset:0\n\t"
    "v_exp_f32 v13, v13\n\t"
    "v_exp_f32 v14, v14\n\t"
    "ds_read_b128 v[60:63], v204 offset:14336\n\t"
    "v_exp_f32 v15, v15\n\t"
    "v_mfma_f32_32x32x16_bf16 v[238:253], v[76:79], v[80:83], v[238:253]\n\t"
    "ds_read_b128 v[64:67], v204 offset:15360\n\t"
    "v_exp_f32 v16, v16\n\t"
    "v_exp_f32 v17, v17\n\t"
    "v_exp_f32 v18, v18\n\t"
    "v_exp_f32 v19, v19\n\t"
    "v_smfmac_f32_32x32x32_bf16 v[156:171], v[152:155], v[8:15], v219\n\t"
    "s_waitcnt lgkmcnt(6)\n\t"
    "v_exp_f32 v20, v20\n\t"
    "v_mfma_f32_32x32x16_bf16 v[0:15], v[136:139], v[128:131], v[96:111]\n\t"
    "v_exp_f32 v21, v21\n\t"
    "v_exp_f32 v22, v22\n\t"
    "v_exp_f32 v23, v23\n\t"
    "v_exp_f32 v24, v24\n\t"
    "v_mfma_f32_32x32x16_bf16 v[188:203], v[84:87], v[92:95], v[188:203]\n\t"
    "s_setprio 2\n\t"
    "v_exp_f32 v25, v25\n\t"
    "v_exp_f32 v26, v26\n\t"
    "v_exp_f32 v27, v27\n\t"
    "v_exp_f32 v28, v28\n\t"
    "v_smfmac_f32_32x32x32_bf16 v[172:187], v[148:151], v[16:23], v219\n\t"
    "v_exp_f32 v29, v29\n\t"
    "v_exp_f32 v30, v30\n\t"
    "v_exp_f32 v31, v31\n\t"
    "v_mfma_f32_32x32x16_bf16 v[238:253], v[88:91], v[92:95], v[238:253]\n\t"
    "s_sub_u32 s59, s59, 1\n\t"
    "s_cmp_lg_u32 s59, 0\n\t"
    "s_waitcnt lgkmcnt(0)\n\t"
    "s_cbranch_scc1 2b\n\t"
    "v_smfmac_f32_32x32x32_bf16 v[172:187], v[152:155], v[24:31], v219\n\t"
    "s_barrier\n\t"
    "ds_read_b128 v[0:3], v222 offset:0\n\t"
    "ds_read_b128 v[4:7], v222 offset:1024\n\t"
    "ds_read_b128 v[8:11], v222 offset:2048\n\t"
    "ds_read_b128 v[12:15], v222 offset:3072\n\t"
    "ds_read2st64_b32 v[16:17], v223 offset1:1\n\t"
    "v_mfma_f32_32x32x16_bf16 v[188:203], v[48:51], v[56:59], v[188:203]\n\t"
    "v_mfma_f32_32x32x16_bf16 v[238:253], v[52:55], v[56:59], v[238:253]\n\t"
    "ds_read_b128 v[80:83], v208 offset:0\n\t"
    "ds_read_b128 v[72:75], v204 offset:24576\n\t"
    "ds_read_b128 v[76:79], v204 offset:25600\n\t"
    "s_nop 1\n\t"
    "s_waitcnt lgkmcnt(3)\n\t"
    "v_add_f32 v164, v164, v16\n\t"
    "v_add_f32 v180, v180, v17\n\t"
    "v_rcp_f32 v16, v164\n\t"
    "v_rcp_f32 v17, v180\n\t"
    "v_add_f32 v156, v156, v0\n\t"
    "v_add_f32 v157, v157, v1\n\t"
    "v_add_f32 v158, v158, v2\n\t"
    "v_add_f32 v159, v159, v3\n\t"
    "v_add_f32 v160, v160, v4\n\t"
    "v_add_f32 v161, v161, v5\n\t"
    "v_add_f32 v162, v162, v6\n\t"
    "v_add_f32 v163, v163, v7\n\t"
    "v_add_f32 v172, v172, v8\n\t"
    "v_add_f32 v173, v173, v9\n\t"
    "v_add_f32 v174, v174, v10\n\t"
    "v_add_f32 v175, v175, v11\n\t"
    "v_add_f32 v176, v176, v12\n\t"
    "v_add_f32 v177, v177, v13\n\t"
    "v_add_f32 v178, v178, v14\n\t"
    "v_add_f32 v179, v179, v15\n\t"
    "v_mfma_f32_32x32x16_bf16 v[188:203], v[60:63], v[68:71], v[188:203]\n\t"
    "v_mul_f32 v156, v156, v16\n\t"
    "v_mul_f32 v157, v157, v16\n\t"
    "v_mul_f32 v158, v158, v16\n\t"
    "v_mul_f32 v159, v159, v16\n\t"
    "v_mul_f32 v160, v160, v16\n\t"
    "v_mul_f32 v161, v161, v16\n\t"
    "v_mul_f32 v162, v162, v16\n\t"
    "v_mul_f32 v163, v163, v16\n\t"
    "v_mul_f32 v172, v172, v17\n\t"
    "v_mul_f32 v173, v173, v17\n\t"
    "v_mul_f32 v174, v174, v17\n\t"
    "v_mul_f32 v175, v175, v17\n\t"
    "v_mul_f32 v176, v176, v17\n\t"
    "v_mul_f32 v177, v177, v17\n\t"
    "v_mul_f32 v178, v178, v17\n\t"
    "v_mul_f32 v179, v179, v17\n\t"
    "v_mfma_f32_32x32x16_bf16 v[238:253], v[64:67], v[68:71], v[238:253]\n\t"
    "v_min_f32 v18, v164, v180\n\t"
    "v_add_f32 v19, v164, v180\n\t"
    "v_add_f32 v20, abs(v156), abs(v157)\n\t"
    "v_add_f32 v21, abs(v160), abs(v161)\n\t"
    "v_add_f32 v22, abs(v172), abs(v173)\n\t"
    "v_add_f32 v23, abs(v176), abs(v177)\n\t"
    "v_add_f32 v20, v20, abs(v158)\n\t"
    "v_add_f32 v21, v21, abs(v162)\n\t"
    "v_add_f32 v22, v22, abs(v174)\n\t"
    "v_add_f32 v23, v23, abs(v178)\n\t"
    "v_add_f32 v20, v20, abs(v159)\n\t"
    "v_add_f32 v21, v21, abs(v163)\n\t"
    "v_add_f32 v22, v22, abs(v175)\n\t"
    "v_add_f32 v23, v23, abs(v179)\n\t"
    "v_cvt_pk_bf16_f32 v0, v156, v157\n\t"
    "v_cvt_pk_bf16_f32 v1, v158, v159\n\t"
    "v_cvt_pk_bf16_f32 v2, v160, v161\n\t"
    "v_cvt_pk_bf16_f32 v3, v162, v163\n\t"
    "v_add_f32 v20, v20, v21\n\t"
    "v_add_f32 v22, v22, v23\n\t"
    "v_add_f32 v19, v19, v20\n\t"
    "v_cvt_pk_bf16_f32 v4, v172, v173\n\t"
    "v_cvt_pk_bf16_f32 v5, v174, v175\n\t"
    "v_cvt_pk_bf16_f32 v6, v176, v177\n\t"
    "v_cvt_pk_bf16_f32 v7, v178, v179\n\t"
    "v_add_f32 v19, v19, v22\n\t"
    "v_cmp_nlt_f32 s[68:69], 0.5, v18\n\t"
    "v_cmp_ngt_f32 vcc, 0x7f000000, v19\n\t"
    "s_nop 3\n\t"
    "s_or_b64 vcc, vcc, s[68:69]\n\t"
    "s_or_b32 s75, vcc_lo, vcc_hi\n\t"
    "s_cbranch_scc1 41f\n\t"
    "global_store_dwordx4 v224, v[0:3], s[64:65] sc1\n\t"
    "global_store_dwordx4 v224, v[4:7], s[66:67] sc1\n\t"
    "41:\n\t"
    "ds_read_b128 v[92:95], v209 offset:0\n\t"
    "ds_read_b128 v[84:87], v204 offset:26624\n\t"
    "ds_read_b128 v[88:91], v204 offset:27648\n\t"
    "s_waitcnt lgkmcnt(0)\n\t"
    "s_branch 9f\n\t"
    "30:\n\t"
    "s_mov_b32 s75, 400000\n\t"
    "302:\n\t"
    "s_sleep 1\n\t"
    "global_load_dwordx2 v[228:229], v226, s[62:63] sc1\n\t"
    "s_waitcnt vmcnt(0)\n\t"
    "v_cmp_ne_u64 vcc, v[228:229], v[230:231]\n\t"
    "s_sub_u32 s75, s75, 1\n\t"
    "s_cbranch_scc1 301b\n\t"
    "s_nop 2\n\t"
    "s_cbranch_vccnz 302b\n\t"
    "s_branch 301b\n\t"
    "20:\n\t"
    "s_mov_b64 exec, 1\n\t"
    "global_store_dwordx2 v227, v[230:231], s[62:63] sc1\n\t"
    "s_mov_b64 exec, -1\n\t"
    "s_branch 21b\n\t"
    "52:\n\t"
    "s_add_u32 m0, s83, 8192\n\t"
    "s_nop 0\n\t"
    "buffer_load_dwordx4 v232, s[84:87], s88 offen lds\n\t"
    "buffer_load_dwordx4 v233, s[84:87], s88 offen offset:1024 lds\n\t"
    "buffer_load_dwordx4 v234, s[84:87], s88 offen offset:2048 lds\n\t"
    "buffer_load_dwordx4 v235, s[84:87], s88 offen offset:3072 lds\n\t"
    "s_branch 62b\n\t"
    "9:\n\t"
    "fused_code_tail:\n\t"
        : P2_HELD, "={v[52:55]}"(ga), "={v[56:63]}"(gb), "={v[64:79]}"(gc), "={v[80:95]}"(gd), "={s75}"(unsafe)
        : "{s55}"(pub), "{s58}"(l0 + LDS_XO), "{s60}"(m1), "{s61}"(m2), "{s[62:63]}"(fw), "{s[76:77]}"(tail), P2_READS, P2_WO, "{s82}"(32768),
          "{s88}"(65536), "{v218}"(vcode), "{v[226:227]}"(fo), "{v[230:231]}"(tag), "{v[222:223]}"(xa), "{v224}"(vl16), "{s[64:65]}"(od),
          "{s[66:67]}"(od2)
        : "memory", "scc", "vcc", "s59", "s68", "s69", "v228", "v229", P2_CLOBBER);
    if (__builtin_expect(__builtin_amdgcn_readfirstlane(unsafe), 0)) {
      __builtin_amdgcn_fence(__ATOMIC_ACQUIRE, "agent");
#pragma unroll 1
      for (int t = 0; t < 2; ++t) {
        const f32x16 o = attn_true_max(kv, *(const bf16x8*)(qp + t * 1024 + vl16), vl16 >> 4);
        st16(ws + WS_O + (size_t)((qt + t) * 32 + head) * 1024 + vl16, pack8(o, 0, __builtin_amdgcn_rcpf(o[8])));
      }
    }
    fo = u32x2{fwait, fo2};
    ST(11);
    asm volatile(
    HZ
    "s_barrier\n\t"
    "v_mfma_f32_32x32x16_bf16 v[188:203], v[72:75], v[80:83], v[188:203]\n\t"
    "ds_read_b128 v[56:59], v206 offset:16384\n\t"
    "v_mfma_f32_32x32x16_bf16 v[238:253], v[76:79], v[80:83], v[238:253]\n\t"
    "ds_read_b128 v[48:51], v204 offset:36864\n\t"
    "ds_read_b128 v[52:55], v204 offset:37888\n\t"
    "v_mfma_f32_32x32x16_bf16 v[188:203], v[84:87], v[92:95], v[188:203]\n\t"
    "ds_read_b128 v[68:71], v207 offset:16384\n\t"
    "v_mfma_f32_32x32x16_bf16 v[238:253], v[88:91], v[92:95], v[238:253]\n\t"
    "ds_read_b128 v[60:63], v204 offset:38912\n\t"
    "ds_read_b128 v[64:67], v204 offset:39936\n\t"
    "s_waitcnt vmcnt(0)\n\t"
    "s_waitcnt lgkmcnt(0)\n\t"
    "s_barrier\n\t"
    "s_cmp_eq_u32 s55, 2\n\t"
    "s_cbranch_scc1 24f\n\t"
    "25:\n\t"
    "v_mfma_f32_32x32x16_bf16 v[188:203], v[48:51], v[56:59], v[188:203]\n\t"
    "ds_read_b128 v[80:83], v208 offset:16384\n\t"
    "v_mfma_f32_32x32x16_bf16 v[238:253], v[52:55], v[56:59], v[238:253]\n\t"
    "ds_read_b128 v[72:75], v204 offset:0\n\t"
    "ds_read_b128 v[76:79], v204 offset:1024\n\t"
    "v_mfma_f32_32x32x16_bf16 v[188:203], v[60:63], v[68:71], v[188:203]\n\t"
    "ds_read_b128 v[92:95], v209 offset:16384\n\t"
    "v_mfma_f32_32x32x16_bf16 v[238:253], v[64:67], v[68:71], v[238:253]\n\t"
    "ds_read_b128 v[84:87], v204 offset:2048\n\t"
    "ds_read_b128 v[88:91], v204 offset:3072\n\t"
    "s_waitcnt lgkmcnt(0)\n\t"
    "s_barrier\n\t"
    "v_mfma_f32_32x32x16_bf16 v[188:203], v[72:75], v[80:83], v[188:203]\n\t"
    "ds_read_b128 v[56:59], v206 offset:32768\n\t"
    "v_mfma_f32_32x32x16_bf16 v[238:253], v[76:79], v[80:83], v[238:253]\n\t"
    "ds_read_b128 v[48:51], v204 offset:12288\n\t"
    "ds_read_b128 v[52:55], v204 offset:13312\n\t"
    "v_mfma_f32_32x32x16_bf16 v[188:203], v[84:87], v[92:95], v[188:203]\n\t"
    "ds_read_b128 v[68:71], v207 offset:32768\n\t"
    "v_mfma_f32_32x32x16_bf16 v[238:253], v[88:91], v[92:95], v[238:253]\n\t"
    "ds_read_b128 v[60:63], v204 offset:14336\n\t"
    "ds_read_b128 v[64:67], v204 offset:15360\n\t"
    "s_waitcnt lgkmcnt(0)\n\t"
    "s_barrier\n\t"
    "v_mfma_f32_32x32x16_bf16 v[188:203], v[48:51], v[56:59], v[188:203]\n\t"
    "ds_read_b128 v[80:83], v208 offset:32768\n\t"
    "v_mfma_f32_32x32x16_bf16 v[238:253], v[52:55], v[56:59], v[238:253]\n\t"
    "ds_read_b128 v[72:75], v204 offset:24576\n\t"
    "ds_read_b128 v[76:79], v204 offset:25600\n\t"
    "v_mfma_f32_32x32x16_bf16 v[188:203], v[60:63], v[68:71], v[188:203]\n\t"
    "ds_read_b128 v[92:95], v209 offset:32768\n\t"
    "v_mfma_f32_32x32x16_bf16 v[238:253], v[64:67], v[68:71], v[238:253]\n\t"
    "ds_read_b128 v[84:87], v204 offset:26624\n\t"
    "ds_read_b128 v[88:91], v204 offset:27648\n\t"
    "s_waitcnt lgkmcnt(0)\n\t"
    "s_barrier\n\t"
    "global_load_dwordx2 v[228:229], v226, s[62:63] sc1\n\t"
    "v_mfma_f32_32x32x16_bf16 v[188:203], v[72:75], v[80:83], v[188:203]\n\t"
    "ds_read_b128 v[56:59], v206 offset:0\n\t"
    "v_mfma_f32_32x32x16_bf16 v[238:253], v[76:79], v[80:83], v[238:253]\n\t"
    "ds_read_b128 v[48:51], v204 offset:36864\n\t"
    "ds_read_b128 v[52:55], v204 offset:37888\n\t"
    "v_mfma_f32_32x32x16_bf16 v[188:203], v[84:87], v[92:95], v[188:203]\n\t"
    "ds_read_b128 v[68:71], v207 offset:0\n\t"
    "v_mfma_f32_32x32x16_bf16 v[238:253], v[88:91], v[92:95], v[238:253]\n\t"
    "ds_read_b128 v[60:63], v204 offset:38912\n\t"
    "ds_read_b128 v[64:67], v204 offset:39936\n\t"
    "s_waitcnt lgkmcnt(0)\n\t"
    "s_barrier\n\t"
    "v_mfma_f32_32x32x16_bf16 v[188:203], v[48:51], v[56:59], v[188:203]\n\t"
    "ds_read_b128 v[80:83], v208 offset:0\n\t"
    "v_mfma_f32_32x32x16_bf16 v[238:253], v[52:55], v[56:59], v[238:253]\n\t"
    "ds_read_b128 v[72:75], v204 offset:0\n\t"
    "ds_read_b128 v[76:79], v204 offset:1024\n\t"
    "v_mfma_f32_32x32x16_bf16 v[188:203], v[60:63], v[68:71], v[188:203]\n\t"
    "ds_read_b128 v[92:95], v209 offset:0\n\t"
    "v_mfma_f32_32x32x16_bf16 v[238:253], v[64:67], v[68:71], v[238:253]\n\t"
    "ds_read_b128 v[84:87], v204 offset:2048\n\t"
    "ds_read_b128 v[88:91], v204 offset:3072\n\t"
    "s_waitcnt lgkmcnt(0)\n\t"
    "s_barrier\n\t"
    "v_mfma_f32_32x32x16_bf16 v[188:203], v[72:75], v[80:83], v[188:203]\n\t"
    "v_mfma_f32_32x32x16_bf16 v[238:253], v[76:79], v[80:83], v[238:253]\n\t"
    "v_mfma_f32_32x32x16_bf16 v[188:203], v[84:87], v[92:95], v[188:203]\n\t"
    "v_mfma_f32_32x32x16_bf16 v[238:253], v[88:91], v[92:95], v[238:253]\n\t"
    "s_waitcnt vmcnt(0)\n\t"
    "v_cmp_ne_u64 vcc, v[228:229], v[230:231]\n\t"
    "s_nop 4\n\t"
    "s_or_b32 s75, vcc_lo, vcc_hi\n\t"
    "s_branch 9f\n\t"
    "24:\n\t"
    "s_mov_b64 exec, 1\n\t"
    "global_store_dwordx2 v227, v[230:231], s[62:63] sc1\n\t"
    "s_mov_b64 exec, -1\n\t"
    "s_branch 25b\n\t"
    "9:\n\t"
    "s_nop 3\n\t"
        : "+{v[188:203]}"(g0), "+{v[238:253]}"(g1), "+{v[48:51]}"(k0), "+{v[52:55]}"(ga), "+{v[56:63]}"(gb), "+{v[64:79]}"(gc), "+{v[80:95]}"(gd),
          "={s75}"(late)
        : "{s55}"(pub), "{s[62:63]}"(fw), P2_READS, "{v[226:227]}"(fo), "{v[230:231]}"(tag)
        : "memory", "scc", "vcc", "v228", "v229");
  }
  ST(10);
  {
    TILE_INDICES(late_block())
    int tz = threadIdx.x;
    asm volatile("" : "+v"(tz));
    const int lane = tz & 63, r = lane & 31, h = lane >> 5;
    const unsigned vl = lane * 16;
    const int ti = w & 1, kq = w >> 1, tt = 2 * tb3 + ti, q = (lane & 15) >> 2;
    if ((qs >> 1) + 2 * g == k2 + 2 * p) {
      f32x4* yl = (f32x4*)(lds + (qs & 1) * 8192) + lane;
#pragma unroll
      for (int j = 0; j < 4; ++j) {
        yl[j * 64] = f32x4{g0[4 * j], g0[4 * j + 1], g0[4 * j + 2], g0[4 * j + 3]};
        yl[256 + j * 64] = f32x4{g1[4 * j], g1[4 * j + 1], g1[4 * j + 2], g1[4 * j + 3]};
      }
    } else {
      const char* yd = ws + WS_Y + (size_t)((2 * tb + (qs >> 1)) * 8 + 2 * e4 + g) * 32768 + np * 8192 + (qs & 1) * 4096;
      st16s(yd, vl, pack8(g0, 0, 1.f));
      st16s(yd, vl + 1024, pack8(g0, 8, 1.f));
      st16s(yd, vl + 2048, pack8(g1, 0, 1.f));
      st16s(yd, vl + 3072, pack8(g1, 8, 1.f));
    }
    const char* op = ws + WS_O + (size_t)(tt * 32 + 8 * kq) * 1024;
    const char* op2 = op + 4096;
    bf16x8 bf[8];
    f32x4 za;
    asm volatile(
        "s_cmp_eq_u32 s75, 0\n\ts_cbranch_scc1 2f\n\ts_mov_b32 s75, 400000\n"
        "1:\n\tglobal_load_dwordx2 v[228:229], %1, %2 sc1\n\ts_waitcnt vmcnt(0)\n\tv_cmp_ne_u64 vcc, v[228:229], %3\n\t"
        "s_sub_u32 s75, s75, 1\n\ts_cbranch_scc1 2f\n\ts_nop 2\n\ts_cbranch_vccz 2f\n\ts_sleep 1\n\ts_branch 1b\n"
        "2:"
        : "+{s75}"(late) : "v"(fwait), "s"(fw), "v"(tag) : "memory", "scc", "vcc", "v228", "v229");
    asm volatile(
        HZ "global_load_dwordx4 %0, %5, %6 sc1\n\tglobal_load_dwordx4 %1, %5, %6 offset:1024 sc1\n\t"
        "global_load_dwordx4 %2, %5, %6 offset:2048 sc1\n\tglobal_load_dwordx4 %3, %5, %6 offset:3072 sc1\n\t"
        "global_load_dwordx4 %4, %5, %7 sc1"
        : "=&v"(bf[0]), "=&v"(bf[1]), "=&v"(bf[2]), "=&v"(bf[3]), "=&v"(bf[4]) : "v"(vl), "s"(op), "s"(op2) : "memory");
#define O_REST                                                                                                                   \
  asm volatile(HZ "global_load_dwordx4 %0, %4, %5 offset:1024 sc1\n\tglobal_load_dwordx4 %1, %4, %5 offset:2048 sc1\n\t"         \
               "global_load_dwordx4 %2, %4, %5 offset:3072 sc1\n\tglobal_load_dwordx4 %3, %6, %7 sc1"                            \
               : "=&v"(bf[5]), "=&v"(bf[6]), "=&v"(bf[7]), "=&v"(za)                                                             \
               : "v"(vl), "s"(op2), "v"(r * 32 + h * 16), "s"(ws + WS_G + tt * 1024) : "memory");
    ST(9);
    bf16x8 af[2][8];
#pragma unroll
    for (int e = 0; e < 2; ++e) {
      const unsigned aa = l0 + (kq < 3 ? LDS_WO + kq * 16384 : LDS_WQ) + (4 * h + q) * 128 + (((e * 4 + ((lane >> 4) & 1) * 2 + ((lane & 3) >> 1)) ^ ((q & 2) << 1)) * 16) + (lane & 1) * 8;
#pragma unroll
      for (int j = 0; j < 8; ++j) af[e][j] = lds_tr2<1024>(aa + j * 2048);
    }
#pragma unroll
    for (int j = 0; j < 8; ++j) asm volatile("" : "+v"(af[0][j]), "+v"(af[1][j]));
    const char* yp = ws + WS_Y + (size_t)(tb3 * 8 + eb) * 32768 + (ti * 4 + kq) * 1024;
    unsigned vy1 = vl + (np ^ 1) * 8192, vy2 = vl + (np ^ 2) * 8192, vy3 = vl + (np ^ 3) * 8192;
    asm volatile("" : "+s"(yp), "+v"(vy1), "+v"(vy2), "+v"(vy3));
    ST(2);
    asm volatile("s_waitcnt vmcnt(5)\n\ts_barrier" ::: "memory");
    set_flag(fw + (F_Y + (tb * 4 + e4) * 4 + np) * FS, tag);
    O_REST
    ST(6);
    if (__builtin_expect(b == 0 && w == 1, 0)) {
#pragma unroll 1
      for (int spin = 0; spin < 400000; ++spin) {
        bool all = true;
#pragma unroll
        for (int j = 0; j < 4; ++j) all &= __hip_atomic_load(fw + (F_Q + j * 64 + lane) * FS, __ATOMIC_RELAXED, __HIP_MEMORY_SCOPE_AGENT) == tag;
        if (__all(all)) break;
      }
    }
    f32x16 acc0 = {}, acc1 = {};
#define P3_STEP(J, N)                                               \
  asm volatile("s_waitcnt vmcnt(" #N ")" : "+v"(bf[J])::"memory"); \
  acc0 = mfma(af[0][J], bf[J], acc0);                               \
  acc1 = mfma(af[1][J], bf[J], acc1);                               \
  __builtin_amdgcn_sched_barrier(0);
    P3_STEP(0, 8) P3_STEP(1, 7) P3_STEP(2, 6) P3_STEP(3, 5) P3_STEP(4, 4) P3_STEP(5, 3) P3_STEP(6, 2) P3_STEP(7, 1)
    asm volatile("s_waitcnt vmcnt(0)" : "+v"(za)::"memory");
    float z = (za[0] + za[1]) + (za[2] + za[3]), z2 = z;
    asm volatile("s_nop 1\n\tv_permlane32_swap_b32 %0, %1" : "+v"(z), "+v"(z2));
    float gt = rbf(__builtin_amdgcn_rcpf(1.f + __expf(-rbf(z + z2))));
    f32x4* fx = (f32x4*)(lds + LDS_XB + ti * 32768 + kq * 2048) + lane;
#pragma unroll
    for (int n = 0; n < 4; ++n) {
      const f32x16& a = n < 2 ? acc0 : acc1;
      const int o = 8 * (n & 1);
      fx[n * 512] = f32x4{a[o], a[o + 1], a[o + 2], a[o + 3]};
      fx[n * 512 + 64] = f32x4{a[o + 4], a[o + 5], a[o + 6], a[o + 7]};
    }
    char* dst = out + (size_t)(tt * 32 + r) * 1024 + (eb * 64 + kq * 16 + h * 8) * 2;
    asm volatile("" : "+v"(dst), "+v"(gt));
    ST(12);
    if (w == 0) await_flags(fw + (F_Y + (tb * 4 + e4) * 4 + (lane & 3)) * FS, tag);
    u32x4 y1, y2, y3;
    asm volatile("s_waitcnt lgkmcnt(0)\n\ts_barrier\n\t" HZ "global_load_dwordx4 %0, %3, %6 sc1\n\tglobal_load_dwordx4 %1, %4, %6 sc1\n\t"
                 "global_load_dwordx4 %2, %5, %6 sc1"
                 : "=&v"(y1), "=&v"(y2), "=&v"(y3) : "v"(vy1), "v"(vy2), "v"(vy3), "s"(yp) : "memory");
    ST(14);
    const f32x4* fy = (const f32x4*)(lds + LDS_XB + ti * 32768 + kq * 8192) + lane;
    const f32x4* yl = (const f32x4*)(lds + (ti * 4 + kq) * 2048) + lane;
    const float g1m = rbf(1.f - gt), gs = p ? -gt : gt;
    f32x4 u = ((fy[0] + fy[128]) + (fy[256] + fy[384])) * g1m + yl[0] * gs, v = ((fy[64] + fy[192]) + (fy[320] + fy[448])) * g1m + yl[64] * gs;
    asm volatile("s_waitcnt vmcnt(0)" : "+v"(y1), "+v"(y2), "+v"(y3), "+v"(u), "+v"(v)::"memory");
    float res[8];
#pragma unroll
    for (int j = 0; j < 8; ++j) {
      const unsigned a = y1[j >> 1], c = y2[j >> 1], d = y3[j >> 1];
      auto half = [&](unsigned t) { return __builtin_bit_cast(float, (j & 1) ? t & 0xffff0000u : t << 16); };
      res[j] = (j < 4 ? u[j & 3] : v[j & 3]) + gs * (half(a) - (half(c) + half(d)));
    }
    const u32x4 pk = {pk2(res[0], res[1]), pk2(res[2], res[3]), pk2(res[4], res[5]), pk2(res[6], res[7])};
    *(u32x4*)dst = swap_halves(pk);
    if (b == 0 && tz == 64) __hip_atomic_store(fw, tag, __ATOMIC_RELAXED, __HIP_MEMORY_SCOPE_AGENT);
  }
  ST(7); ST_FLUSH(0);
  asm volatile("fused_code_end:");
}

extern "C" void kernel_launch(void* const* d_in, const int* in_sizes, int n_in, void* d_out, int out_size, void* d_ws, size_t ws_size,
                              hipStream_t stream) {
  (void)hipFuncSetAttribute((const void*)fused, hipFuncAttributeMaxDynamicSharedMemorySize, LDS_BYTES);
  fused<<<256, 512, LDS_BYTES, stream>>>((const char*)d_in[0], (const char*)d_in[1], (const char*)d_in[2], (const char*)d_in[3], (const char*)d_in[4],
                                         (const char*)d_in[6], (const char*)d_in[7], (const char*)d_in[8], (char*)d_ws, (char*)d_out);
}
```
